```python
import torch

def _softmax(x, axis=-1):
    return torch.softmax(x, dim=axis)
def _gelu(x):
    return torch.nn.functional.gelu(x, approximate="tanh")
def _silu(x):
    return torch.nn.functional.silu(x)
def _sigmoid(x):
    return torch.sigmoid(x)
def _layernorm(x, axis=-1, eps=1e-5):
    m = x.mean(dim=axis, keepdim=True)
    v = x.var(dim=axis, keepdim=True, unbiased=False)
    return (x - m) / torch.sqrt(v + eps)
def _scatter_add(tgt, idx, src, axis=0):
    return tgt.clone().index_add_(axis, idx, src)
def _segment_sum(src, seg, n):
    out = torch.zeros((n,) + src.shape[1:], dtype=src.dtype)
    return out.index_add_(0, seg, src)
def _pad(x, pw):
    flat = tuple(v for pair in reversed(pw) for v in pair)
    return torch.nn.functional.pad(x, flat)

kernel_name = 'sequential_preLN__ssm_conv_h3__swiglu_ffn__prompt'

def setup_inputs():

    _w = (lambda a: (lambda t: t.to(torch.bfloat16) if t.is_floating_point() else t)(torch.as_tensor(a)))
    def _A_setup():
        B, S, D = 1, 2048, 512
        ks = 4
        return {
            "q": _w((lambda *s: torch.randn(*s, dtype=torch.bfloat16) * 0.05)(B, S, D)),
            "k": _w((lambda *s: torch.randn(*s, dtype=torch.bfloat16) * 0.4)(B, S, D)),
            "v": _w((lambda *s: torch.randn(*s, dtype=torch.bfloat16) * 0.4)(B, S, D)),
            "shift_k": _w((lambda *s: torch.randn(*s, dtype=torch.bfloat16) * 0.4)(D, ks) * 0.5),
            "ssm_k": _w((lambda *s: torch.randn(*s, dtype=torch.bfloat16) * 0.4)(D, S) * (1.0 / S ** 0.5)),
            "ssm_D": _w((lambda *s: torch.randn(*s, dtype=torch.bfloat16) * 0.4)(D)),
        }
    _A_in = _A_setup()

    def _B_setup():
        B, S, D, F = 1, 2048, 512, 1792
        return {
            "x": _w((lambda *s: torch.randn(*s, dtype=torch.bfloat16) * 0.05)(B, S, D)),
            "w_gate": _w((lambda *s: torch.randn(*s, dtype=torch.bfloat16) * 0.05)(D, F)),
            "w_up": _w((lambda *s: torch.randn(*s, dtype=torch.bfloat16) * 0.05)(D, F)),
            "w_down": _w((lambda *s: torch.randn(*s, dtype=torch.bfloat16) * 0.05)(F, D)),
        }
    _B_in = _B_setup()

    return {
        "A_q": _A_in["q"],
        "A_k": _A_in["k"],
        "A_v": _A_in["v"],
        "A_shift_k": _A_in["shift_k"],
        "A_ssm_k": _A_in["ssm_k"],
        "A_ssm_D": _A_in["ssm_D"],
        "B_x": _B_in["x"],
        "B_w_gate": _B_in["w_gate"],
        "B_w_up": _B_in["w_up"],
        "B_w_down": _B_in["w_down"],
    }

def reference(A_q, A_k, A_v, A_shift_k, A_ssm_k, A_ssm_D, B_x, B_w_gate, B_w_up, B_w_down):
    def _A_ref(q, k, v, shift_k, ssm_k, ssm_D):
        B, S, D = q.shape
        ks = shift_k.shape[-1]
        k_pad = _pad(k, ((0, 0), (ks - 1, 0), (0, 0)))
        k_shift = torch.zeros_like(k)
        for t in range(ks):
            k_shift = k_shift + k_pad[:, t:t + S, :] * shift_k[:, ks - 1 - t][None, None, :]
        kv = k_shift * v
        L = 2 * S
        kv_pad = _pad(kv, ((0, 0), (0, S), (0, 0)))
        kf_pad = _pad(ssm_k, ((0, 0), (0, S)))
        kv_f = (lambda x, axes=(-2,-1): torch.fft.rfft2(x.float(), dim=axes))(kv_pad[:, None, :, :], axes=(1, 2))
        kf_f = (lambda x, axes=(-2,-1): torch.fft.rfft2(x.float(), dim=axes))(kf_pad[:, None, :], axes=(1, 2))
        kf_f = (lambda x, *d: x.permute(*d))(kf_f, 1, 2, 0)
        y_f = kv_f * kf_f[None, :, :, :]
        y = (lambda x, s, axes=(-2,-1): torch.fft.irfft2(x, s=s, dim=axes).to(torch.bfloat16))(y_f, (1, L), axes=(1, 2))[:, 0, :S, :]
        y = y + kv * ssm_D[None, None, :]
        return q * y

    def _B_ref(x, w_gate, w_up, w_down):
        g = _silu(x @ w_gate)
        u = x @ w_up
        return (g * u) @ w_down

    x = A_q
    h = x + _A_ref(_layernorm(x, axis=-1), A_k, A_v, A_shift_k, A_ssm_k, A_ssm_D)
    h2 = h + _B_ref(_layernorm(h, axis=-1), B_w_gate, B_w_up, B_w_down)
    return h2
```

```cpp
#include <hip/hip_runtime.h>
#include <hip/hip_bf16.h>
#include <stdint.h>

typedef unsigned short u16;
__device__ __forceinline__ float bf2f(u16 v) { return __uint_as_float(((unsigned)v) << 16); }
__device__ __forceinline__ u16 f2bf(float f) { __hip_bfloat16 b = __float2bfloat16(f); return *reinterpret_cast<u16*>(&b); }
__device__ __forceinline__ float rbf(float f) { return bf2f(f2bf(f)); }

constexpr int S = 2048, D = 512, F = 1792;

__global__ void k_kv(const u16* k, const u16* v, const u16* shift_k, u16* kv) {
  int i = blockIdx.x * blockDim.x + threadIdx.x;
  int s = i / D, d = i % D;
  float acc = 0.f;
  for (int t = 0; t < 4; ++t) {
    int ss = s + t - 3;
    float kk = ss >= 0 ? bf2f(k[ss * D + d]) : 0.f;
    acc = rbf(acc + rbf(kk * bf2f(shift_k[d * 4 + 3 - t])));
  }
  kv[i] = f2bf(acc * bf2f(v[i]));
}

__global__ void k_conv(const u16* kv, const u16* ssm_k, const u16* ssm_D, u16* y) {
  int i = blockIdx.x * blockDim.x + threadIdx.x;
  int s = i / D, d = i % D;
  float acc = 0.f;
  for (int j = 0; j <= s; ++j) acc += bf2f(kv[(s - j) * D + d]) * bf2f(ssm_k[d * S + j]);
  float r = rbf(acc);
  y[i] = f2bf(r + rbf(bf2f(kv[i]) * bf2f(ssm_D[d])));
}

__device__ float block_sum(float v, float* sm) {
  for (int o = 32; o > 0; o >>= 1) v += __shfl_xor(v, o);
  __syncthreads();
  if ((threadIdx.x & 63) == 0) sm[threadIdx.x >> 6] = v;
  __syncthreads();
  float r = 0.f;
  for (int w = 0; w < (int)(blockDim.x >> 6); ++w) r += sm[w];
  return r;
}

__global__ void k_h(const u16* x, const u16* y, u16* h, u16* hn) {
  __shared__ float sm[8];
  int s = blockIdx.x, d = threadIdx.x;
  float xv = bf2f(x[s * D + d]);
  float m = block_sum(xv, sm) / D;
  float var = block_sum((xv - m) * (xv - m), sm) / D;
  float q = rbf(rbf(xv - rbf(m)) / rbf(sqrtf(rbf(rbf(var) + 1e-5f))));
  float hv = rbf(xv + rbf(q * bf2f(y[s * D + d])));
  h[s * D + d] = f2bf(hv);
  float m2 = block_sum(hv, sm) / D;
  float v2 = block_sum((hv - m2) * (hv - m2), sm) / D;
  hn[s * D + d] = f2bf(rbf(hv - rbf(m2)) / rbf(sqrtf(rbf(rbf(v2) + 1e-5f))));
}

__global__ void k_g1(const u16* hn, const u16* wg, const u16* wu, u16* act) {
  int i = blockIdx.x * blockDim.x + threadIdx.x;
  int s = i / F, f = i % F;
  float g = 0.f, u = 0.f;
  for (int k = 0; k < D; ++k) { float a = bf2f(hn[s * D + k]); g += a * bf2f(wg[k * F + f]); u += a * bf2f(wu[k * F + f]); }
  g = rbf(g); u = rbf(u);
  float sg = rbf(g / (1.f + __expf(-g)));
  act[i] = f2bf(sg * u);
}

__global__ void k_g2(const u16* act, const u16* wd, const u16* h, u16* out) {
  int i = blockIdx.x * blockDim.x + threadIdx.x;
  int s = i / D, d = i % D;
  float acc = 0.f;
  for (int k = 0; k < F; ++k) acc += bf2f(act[s * F + k]) * bf2f(wd[k * D + d]);
  out[i] = f2bf(bf2f(h[i]) + rbf(acc));
}

extern "C" void kernel_launch(void* const* d_in, const int* in_sizes, int n_in,
                              void* d_out, int out_size, void* d_ws, size_t ws_size,
                              hipStream_t stream) {
  const u16* q = (const u16*)d_in[0];
  const u16* k = (const u16*)d_in[1];
  const u16* v = (const u16*)d_in[2];
  const u16* shift_k = (const u16*)d_in[3];
  const u16* ssm_k = (const u16*)d_in[4];
  const u16* ssm_D = (const u16*)d_in[5];
  const u16* wg = (const u16*)d_in[7];
  const u16* wu = (const u16*)d_in[8];
  const u16* wd = (const u16*)d_in[9];
  u16* ws = (u16*)d_ws;
  u16* kv = ws; u16* y = kv + S * D; u16* h = y + S * D; u16* hn = h + S * D; u16* act = hn + S * D;
  k_kv<<<S * D / 256, 256, 0, stream>>>(k, v, shift_k, kv);
  k_conv<<<S * D / 256, 256, 0, stream>>>(kv, ssm_k, ssm_D, y);
  k_h<<<S, 512, 0, stream>>>(q, y, h, hn);
  k_g1<<<S * F / 256, 256, 0, stream>>>(hn, wg, wu, act);
  k_g2<<<S * D / 256, 256, 0, stream>>>(act, wd, h, (u16*)d_out);
}
```

```cpp
#include <hip/hip_runtime.h>
#include <stdint.h>

typedef unsigned short u16;
typedef unsigned u32;
typedef unsigned long long u64;
typedef u32 v4u __attribute__((ext_vector_type(4)));
typedef u32 v2u __attribute__((ext_vector_type(2)));
typedef float f4 __attribute__((ext_vector_type(4)));
typedef float f16v __attribute__((ext_vector_type(16)));
typedef __bf16 v8bf __attribute__((ext_vector_type(8)));
typedef __bf16 v2bf __attribute__((ext_vector_type(2)));
typedef short v4s __attribute__((ext_vector_type(4)));
typedef short v8s __attribute__((ext_vector_type(8)));
#define LAS __attribute__((address_space(3)))
#define DEVI __device__ __forceinline__

constexpr int S = 2048, D = 512, F = 1792;
constexpr float LN_EPS = 1e-5f;

constexpr size_t WS_U = 4096;
constexpr size_t WS_XP = WS_U + (2u << 20);
constexpr size_t WS_DUP = WS_XP + (2u << 20);
constexpr size_t WS_HF = WS_DUP + (2u << 20);
constexpr size_t WS_ACT = WS_HF + (2u << 20);
constexpr size_t WS_XST = WS_ACT + (size_t)224 * 2048 * 16;
constexpr size_t WS_CSP = WS_XST + 16384;
constexpr size_t WS_STAMP = WS_CSP + 65536;

#define MAP2 1
#define MAP3 1

#define STAMP(k, e) do {} while (0)
#define STAMP_ARG
#define STAMP_PASS

DEVI float blo(u32 v) { return __uint_as_float(v << 16); }
DEVI float bhi(u32 v) { return __uint_as_float(v & 0xffff0000u); }
DEVI u32 pk2(float a, float b) { v2bf p = {(__bf16)a, (__bf16)b}; return __builtin_bit_cast(u32, p); }
DEVI v4u mk_srd(const void* p, u32 bytes) {
  u64 a = (u64)p;
  v4u r = {(u32)a, (u32)(a >> 32) & 0xffffu, bytes, 0x00020000u};
  return r;
}
DEVI float dot2(u32 a, u32 b, float c) {
  return __builtin_amdgcn_fdot2_f32_bf16(__builtin_bit_cast(v2bf, a), __builtin_bit_cast(v2bf, b), c, false);
}
#define GLD128(dst, srd, voff, soff) \
  asm volatile("buffer_load_dwordx4 %0, %1, %2, %3 offen" : "=v"(dst) : "v"(voff), "s"(srd), "s"(soff) : "memory")
#define GLD128O(dst, srd, voff, soff, imm) \
  asm volatile("buffer_load_dwordx4 %0, %1, %2, %3 offen offset:%4" : "=v"(dst) : "v"(voff), "s"(srd), "s"(soff), "i"(imm) : "memory")
#define GLD32(dst, srd, voff, soff) \
  asm volatile("buffer_load_dword %0, %1, %2, %3 offen" : "=v"(dst) : "v"(voff), "s"(srd), "s"(soff) : "memory")
#define DMA16(ldsaddr, srd, voff, soff, imm) \
  asm volatile("s_mov_b32 m0, %0\n\ts_nop 0\n\tbuffer_load_dwordx4 %1, %2, %3 offen offset:%4 lds" \
               :: "s"(ldsaddr), "v"(voff), "s"(srd), "s"(soff), "i"(imm) : "memory")

DEVI v8bf tr_frag(u32 a0, u32 a1) {
  v4s lo = __builtin_amdgcn_ds_read_tr16_b64_v4i16((LAS v4s*)(uintptr_t)a0);
  v4s hi = __builtin_amdgcn_ds_read_tr16_b64_v4i16((LAS v4s*)(uintptr_t)a1);
  v8s r = __builtin_shufflevector(lo, hi, 0, 1, 2, 3, 4, 5, 6, 7);
  return __builtin_bit_cast(v8bf, r);
}
DEVI v4u lds128(u32 a) { return *(LAS v4u*)(uintptr_t)a; }
DEVI f4 ldsf4(u32 a) { return *(LAS f4*)(uintptr_t)a; }
DEVI void sts128(u32 a, v4u v) { *(LAS v4u*)(uintptr_t)a = v; }
DEVI void stsf4(u32 a, f4 v) { *(LAS f4*)(uintptr_t)a = v; }

__global__ __launch_bounds__(256) void k0_prep(const u16* __restrict__ x, const u16* __restrict__ kin, const u16* __restrict__ vin,
                                               const u16* __restrict__ shift_k, const u16* __restrict__ ssm_k,
                                               const u16* __restrict__ ssm_D, const u16* __restrict__ wg,
                                               const u16* __restrict__ wu, const u16* __restrict__ wd, char* __restrict__ ws STAMP_ARG) {
  __shared__ __attribute__((aligned(16))) u32 T[64 * 36];
  __shared__ float CS[4 * 64];
  STAMP(0, 0);
  const int bid = blockIdx.x, tid = threadIdx.x, lane = tid & 63, w = tid >> 6;
  const int cg = bid & 7, zb = bid >> 3;
  const int p = tid & 31, o = tid >> 5;
  const int P = cg * 32 + p;
  const int r0 = zb * 64 + o * 8;
  const u32* k32 = (const u32*)kin; const u32* v32 = (const u32*)vin; const u32* x32 = (const u32*)x;
  u32 kk[11], vv[8], xx[8];
#pragma unroll
  for (int i = 0; i < 11; ++i) { int row = r0 - 3 + i; row = row < 0 ? 0 : row; kk[i] = k32[row * 256 + P]; }
#pragma unroll
  for (int i = 0; i < 8; ++i) vv[i] = v32[(r0 + i) * 256 + P];
#pragma unroll
  for (int i = 0; i < 8; ++i) xx[i] = x32[(r0 + i) * 256 + P];
  const v4u sk = *(const v4u*)(shift_k + P * 8);
  const u32 dd = ((const u32*)ssm_D)[P];
  v4u xr[2];
#pragma unroll
  for (int i = 0; i < 2; ++i) xr[i] = *(const v4u*)(x + (size_t)(bid * 8 + w * 2 + i) * D + lane * 8);
  v4u wv[4];
  const int cc = bid % 56, kq = bid / 56;
  if (bid < 224) {
    const u16* wsrc = (cc < 28 ? wg + cc * 64 : wu + (cc - 28) * 64) + (size_t)(kq * 128 + (tid >> 3)) * F + (tid & 7) * 8;
#pragma unroll
    for (int i = 0; i < 4; ++i) wv[i] = *(const v4u*)(wsrc + (size_t)i * 32 * F);
  } else {
#pragma unroll
    for (int i = 0; i < 4; ++i) wv[i] = v4u{0, 0, 0, 0};
  }
  v4u pf0 = *(const v4u*)(ssm_k + (size_t)(cg * 32 + zb) * 4096 + tid * 8);
  v4u pf1 = *(const v4u*)(ssm_k + (size_t)(cg * 32 + zb) * 4096 + 2048 + tid * 8);
  v4u pf2 = *(const v4u*)(wd + (size_t)bid * 3584 + (tid < 224 ? tid : 0) * 8);
  v4u pf3 = *(const v4u*)(wd + (size_t)bid * 3584 + 1792 + (tid < 224 ? tid : 0) * 8);

  if (r0 == 0) { kk[0] = 0; kk[1] = 0; kk[2] = 0; }
  {
    v4u* dst = (v4u*)(ws + WS_XP + (size_t)P * 8192 + r0 * 4);
    dst[0] = v4u{xx[0], xx[1], xx[2], xx[3]};
    dst[1] = v4u{xx[4], xx[5], xx[6], xx[7]};
  }
  const float wa0 = blo(sk.x), wa1 = bhi(sk.x), wa2 = blo(sk.y), wa3 = bhi(sk.y);
  const float wb0 = blo(sk.z), wb1 = bhi(sk.z), wb2 = blo(sk.w), wb3 = bhi(sk.w);
  const float da = blo(dd), db = bhi(dd);
  u32 uu[8], du[8];
#pragma unroll
  for (int i = 0; i < 8; ++i) {
    float a = blo(kk[i]) * wa3; a = fmaf(blo(kk[i + 1]), wa2, a); a = fmaf(blo(kk[i + 2]), wa1, a); a = fmaf(blo(kk[i + 3]), wa0, a);
    float b = bhi(kk[i]) * wb3; b = fmaf(bhi(kk[i + 1]), wb2, b); b = fmaf(bhi(kk[i + 2]), wb1, b); b = fmaf(bhi(kk[i + 3]), wb0, b);
    uu[i] = pk2(a * blo(vv[i]), b * bhi(vv[i]));
    du[i] = pk2(blo(uu[i]) * da, bhi(uu[i]) * db);
  }
  {
    v4u* dst = (v4u*)(ws + WS_DUP + (size_t)P * 8192 + r0 * 4);
    dst[0] = v4u{du[0], du[1], du[2], du[3]};
    dst[1] = v4u{du[4], du[5], du[6], du[7]};
  }
#pragma unroll
  for (int r = 0; r < 4; ++r) {
    T[(2 * p) * 36 + r * 8 + o] = (uu[r] & 0xffffu) | (uu[4 + r] << 16);
    T[(2 * p + 1) * 36 + r * 8 + o] = (uu[r] >> 16) | (uu[4 + r] & 0xffff0000u);
  }
#pragma unroll
  for (int i = 0; i < 2; ++i) {
    float e[8] = {blo(xr[i].x), bhi(xr[i].x), blo(xr[i].y), bhi(xr[i].y), blo(xr[i].z), bhi(xr[i].z), blo(xr[i].w), bhi(xr[i].w)};
    float s = 0.f;
#pragma unroll
    for (int j = 0; j < 8; ++j) s += e[j];
#pragma unroll
    for (int m = 1; m < 64; m <<= 1) s += __shfl_xor(s, m);
    const float mu = s * (1.f / D);
    float q = 0.f;
#pragma unroll
    for (int j = 0; j < 8; ++j) { float t = e[j] - mu; q = fmaf(t, t, q); }
#pragma unroll
    for (int m = 1; m < 64; m <<= 1) q += __shfl_xor(q, m);
    if (lane == 0) ((float2*)(ws + WS_XST))[bid * 8 + w * 2 + i] = make_float2(mu, rsqrtf(q * (1.f / D) + LN_EPS));
  }
  {
    float c[8];
#pragma unroll
    for (int j = 0; j < 4; ++j) {
      c[2 * j] = (blo(wv[0][j]) + blo(wv[1][j])) + (blo(wv[2][j]) + blo(wv[3][j]));
      c[2 * j + 1] = (bhi(wv[0][j]) + bhi(wv[1][j])) + (bhi(wv[2][j]) + bhi(wv[3][j]));
    }
#pragma unroll
    for (int m = 8; m < 64; m <<= 1)
#pragma unroll
      for (int j = 0; j < 8; ++j) c[j] += __shfl_xor(c[j], m);
    if (lane < 8) {
#pragma unroll
      for (int j = 0; j < 8; ++j) CS[w * 64 + lane * 8 + j] = c[j];
    }
  }
  __syncthreads();
  {
    const int ch = tid >> 2, rho = tid & 3;
    const v4u a = *(const v4u*)&T[ch * 36 + rho * 8], b = *(const v4u*)&T[ch * 36 + rho * 8 + 4];
    v4u* dst = (v4u*)(ws + WS_U + (size_t)(cg * 64 + ch) * 4096 + rho * 1024 + zb * 32);
    dst[0] = a; dst[1] = b;
  }
  if (bid < 224 && tid < 64)
    ((float*)(ws + WS_CSP))[kq * 2 * F + cc * 64 + tid] = (CS[tid] + CS[64 + tid]) + (CS[128 + tid] + CS[192 + tid]);
  asm volatile("" :: "v"(pf0), "v"(pf1), "v"(pf2), "v"(pf3));
  STAMP(0, 1);
}

constexpr int K1_K = 0;
constexpr int K1_U = 8448;
constexpr int K1_Y = K1_U + 2 * 9472;
constexpr int K1_C = K1_Y + 16384;
constexpr int K1_R = K1_C + 2 * 576 * 16;
constexpr int K1_LDS = K1_R + 8192;

template <int T0, int T1, int T2, int T3>
DEVI void conv_tiles(u32 abase, u32 ubase, u32 ybase, u32 cbase) {
  constexpr int TT[4] = {T0, T1, T2, T3};
  f4 acc[4][4];
#pragma unroll
  for (int t = 0; t < 4; ++t)
#pragma unroll
    for (int r = 0; r < 4; ++r) acc[t][r] = f4{0.f, 0.f, 0.f, 0.f};
  v8bf A[2 * T3 + 2];
#pragma unroll
  for (int dl = 2 * T3; dl >= -1; --dl) {
    const int inew = 2 * T3 - dl;
    A[inew] = tr_frag(abase + 256 * inew + 32, abase + 256 * inew);
#pragma unroll
    for (int r = 0; r < 4; ++r) {
      const v8bf B = __builtin_bit_cast(v8bf, lds128(ubase + r * 1152 + 64 * (dl + 1)));
#pragma unroll
      for (int t = 3; t >= 0; --t)
        if (dl <= 2 * TT[t]) acc[t][r] = __builtin_amdgcn_mfma_f32_16x16x32_bf16(A[2 * TT[t] - dl], B, acc[t][r], 0, 0, 0);
    }
  }
#pragma unroll
  for (int t = 0; t < 4; ++t) {
    f4 own = {acc[t][0][0], acc[t][0][1] + acc[t][1][0], acc[t][0][2] + acc[t][1][1] + acc[t][2][0],
              (acc[t][0][3] + acc[t][1][2]) + (acc[t][2][1] + acc[t][3][0])};
    f4 car = {acc[t][1][3] + acc[t][2][2] + acc[t][3][1], acc[t][2][3] + acc[t][3][2], acc[t][3][3], 0.f};
    stsf4(ybase + TT[t] * 1024, own);
    stsf4(cbase + TT[t] * 1024, car);
  }
}

__global__ __launch_bounds__(256) void k1_conv(const u16* __restrict__ ssm_k, char* __restrict__ ws STAMP_ARG) {
  __shared__ __attribute__((aligned(256))) char sm[K1_LDS];
  STAMP(1, 0);
  const int bid = blockIdx.x, tid = threadIdx.x, lane = tid & 63;
  const int w = __builtin_amdgcn_readfirstlane(tid >> 6);
  const int P = (bid & 7) * 32 + (bid >> 3);
  const int e = w >> 1;
  const u32 sb = (u32)(uintptr_t)(LAS char*)sm;
  const v4u srdK = mk_srd(ssm_k, 512u * 4096u);
  const v4u srdW = mk_srd(ws, 0x7fffffffu);
  const u32 l16 = lane * 16;
  {
    const u32 uso = (u32)WS_U + (2 * P + e) * 4096;
    const u32 ul = sb + K1_U + e * 9472 + 128;
    if ((w & 1) == 0) {
      const u32 kso = (2 * P + e) * 4096, kl = sb + K1_K + e * 4128 + 32;
      DMA16(kl, srdK, l16, kso, 0); DMA16(kl, srdK, l16, kso, 1024); DMA16(kl, srdK, l16, kso, 2048); DMA16(kl, srdK, l16, kso, 3072);
      DMA16(ul, srdW, l16, uso, 0); DMA16(ul + 128, srdW, l16, uso, 1024);
    } else {
      DMA16(ul + 256, srdW, l16, uso, 2048); DMA16(ul + 384, srdW, l16, uso, 3072);
      DMA16(ul + 4736, srdW, l16, uso - 8, 0); DMA16(ul + 4736 + 128, srdW, l16, uso - 8, 1024);
      DMA16(ul + 4736 + 256, srdW, l16, uso - 8, 2048); DMA16(ul + 4736 + 384, srdW, l16, uso - 8, 3072);
    }
  }
  const int n = lane & 15, g = lane >> 4;
  const u32 p4 = 4 * n + g;
  v4u xq[2], dq[2], st[2][2];
#pragma unroll
  for (int i = 0; i < 2; ++i) {
    const u32 so = (u32)P * 8192 + (2 * w + i) * 1024;
    GLD128(xq[i], srdW, p4 * 16, (u32)WS_XP + so);
    GLD128(dq[i], srdW, p4 * 16, (u32)WS_DUP + so);
    GLD128(st[i][0], srdW, p4 * 32, (u32)WS_XST + (2 * w + i) * 2048);
    GLD128O(st[i][1], srdW, p4 * 32, (u32)WS_XST + (2 * w + i) * 2048, 16);
  }
  {
    const v4u z = {0, 0, 0, 0};
    if (tid < 128) {
      const int arr = tid >> 3;
      sts128(sb + K1_U + (arr >> 3) * 9472 + ((arr >> 2) & 1) * 4736 + (arr & 3) * 1152 + (tid & 7) * 16, z);
    } else if (tid < 132) {
      sts128(sb + K1_K + ((tid >> 1) & 1) * 4128 + (tid & 1) * 16, z);
    } else if (tid < 134) {
      sts128(sb + K1_C + (tid & 1) * 576 * 16, z);
    }
  }
  asm volatile("s_waitcnt vmcnt(8)" ::: "memory");
  if ((w & 1) && lane < 4) *(LAS v2u*)(uintptr_t)(sb + K1_U + e * 9472 + 4736 + lane * 1152 + 128) = v2u{0, 0};
  asm volatile("s_waitcnt lgkmcnt(0)\n\ts_barrier" ::: "memory");
  {
    const int q = n >> 2, pp = n & 3;
    const u32 abase = sb + K1_K + e * 4128 + 32 + 64 * g + 8 * (pp - q);
    const u32 ubase = sb + K1_U + e * 9472 + ((n & 1) ? 120 : 4736 + 128) + 8 * n - 16 * g - 64;
    const u32 ybase = sb + K1_Y + e * 8192 + lane * 16;
    const u32 cbase = sb + K1_C + e * 576 * 16 + (g < 3 ? (lane + 16) : (n < 15 ? n + 1 : 64)) * 16;
    if (w & 1) conv_tiles<1, 2, 5, 6>(abase, ubase, ybase, cbase);
    else conv_tiles<0, 3, 4, 7>(abase, ubase, ybase, cbase);
  }
  asm volatile("s_waitcnt vmcnt(0) lgkmcnt(0)\n\ts_barrier"
               : "+v"(xq[0]), "+v"(xq[1]), "+v"(dq[0]), "+v"(dq[1]), "+v"(st[0][0]), "+v"(st[0][1]), "+v"(st[1][0]), "+v"(st[1][1]) :: "memory");
  u32* hF = (u32*)(ws + WS_HF) + (size_t)(P >> 2) * 8192 + (P & 3);
#pragma unroll
  for (int i = 0; i < 2; ++i) {
    const int tile = 2 * w + i;
    const u32 ya = sb + K1_Y + tile * 1024 + lane * 16, ca = sb + K1_C + tile * 1024 + lane * 16;
    const f4 y0 = ldsf4(ya) + ldsf4(ca), y1 = ldsf4(ya + 8192) + ldsf4(ca + 576 * 16);
    v4u hv;
#pragma unroll
    for (int r = 0; r < 4; ++r) {
      const float mu = __uint_as_float(st[i][r >> 1][(r & 1) * 2]), rs = __uint_as_float(st[i][r >> 1][(r & 1) * 2 + 1]);
      const float xa = blo(xq[i][r]), xb = bhi(xq[i][r]);
      const float ha = fmaf((xa - mu) * rs, y0[r] + blo(dq[i][r]), xa);
      const float hb = fmaf((xb - mu) * rs, y1[r] + bhi(dq[i][r]), xb);
      hv[r] = pk2(ha, hb);
    }
    const u32 rb = sb + K1_R + w * 2048 + i * 1024;
    sts128(rb + p4 * 16, hv);
#pragma unroll
    for (int r = 0; r < 4; ++r) {
      const u32 val = *(LAS u32*)(uintptr_t)(rb + (r * 64 + lane) * 4);
      hF[(tile * 256 + r * 64 + lane) * 4] = val;
    }
  }
  STAMP(1, 1);
}

constexpr int K2_CS = 131072;
constexpr int K2_LDS = K2_CS + 512;

__global__ __launch_bounds__(512, 2) void k2_gemm1(const u16* __restrict__ wg, const u16* __restrict__ wu, char* __restrict__ ws STAMP_ARG) {
  __shared__ __attribute__((aligned(256))) char sm[K2_LDS];
  STAMP(2, 0);
  const int bid = blockIdx.x, tid = threadIdx.x, lane = tid & 63;
  const int w = __builtin_amdgcn_readfirstlane(tid >> 6);
  const int xcd = bid & 7, idx = bid >> 3;
  int rb, sl;
  if (MAP2 == 0) { rb = xcd; sl = idx; }
  else if (MAP2 == 1) { rb = (xcd & 6) + idx / 14; sl = 14 * (xcd & 1) + idx % 14; }
  else { rb = (xcd & 4) + idx / 7; sl = 7 * (xcd & 3) + idx % 7; }
  const u32 sb = (u32)(uintptr_t)(LAS char*)sm;
  const v4u srdW = mk_srd(ws, 0x7fffffffu);
  const v4u srdM = mk_srd((w & 1) ? wu : wg, (u32)D * F * 2);
  const int hh = lane >> 5;
  const int srow = rb * 256 + w * 32 + (lane & 31);
  u32 cp[4];
  {
    const u32 vo = ((tid >> 6) & 1) * F * 4 + (sl * 64 + lane) * 4;
#pragma unroll
    for (int i = 0; i < 4; ++i) GLD32(cp[i], srdW, vo, (u32)WS_CSP + i * 2 * F * 4);
  }
  v4u h[32];
  {
    const int kk = lane >> 3, c8 = lane & 7;
    const int nl = (c8 >> 1) ^ ((((kk >> 1) & 1) << 1) | (w & 1));
    const u32 wvo = kk * (F * 2) + sl * 128 + nl * 32 + (c8 & 1) * 16;
    const u32 hvo = (hh * 2048 + srow) * 16;
#pragma unroll
    for (int m = 0; m < 16; ++m) {
      const int G = 4 * m + (w >> 1);
      DMA16(sb + 2048 * G + 1024 * (w & 1), srdM, wvo, G * (8 * F * 2), 0);
      GLD128(h[2 * m], srdW, hvo, (u32)WS_HF + (4 * m) * 32768);
      GLD128(h[2 * m + 1], srdW, hvo, (u32)WS_HF + (4 * m + 2) * 32768);
    }
  }
  u32 wa[4];
  {
    const int blk = (lane >> 4) & 1, q = (lane & 15) >> 2, pp = lane & 3;
    const int sw = (((q >> 1) & 1) << 1) | blk;
#pragma unroll
    for (int n = 0; n < 4; ++n) wa[n] = sb + 2048 * hh + 1024 * blk + 128 * q + 8 * pp + 32 * (n ^ sw);
  }
  f16v acc[4];
#pragma unroll
  for (int n = 0; n < 4; ++n)
#pragma unroll
    for (int i = 0; i < 16; ++i) acc[n][i] = 0.f;
  float s1 = 0.f, s2 = 0.f;
  asm volatile("s_waitcnt vmcnt(45)\n\ts_barrier" : "+v"(h[0]), "+v"(h[1]), "+v"(cp[0]), "+v"(cp[1]), "+v"(cp[2]), "+v"(cp[3]) :: "memory");
  if (tid < 128)
    *(LAS float*)(uintptr_t)(sb + K2_CS + tid * 4) =
        (__uint_as_float(cp[0]) + __uint_as_float(cp[1])) + (__uint_as_float(cp[2]) + __uint_as_float(cp[3]));
  v8bf Wf[2][4];
#pragma unroll
  for (int n = 0; n < 4; ++n) Wf[0][n] = tr_frag(wa[n], wa[n] + 512);
#pragma unroll
  for (int j = 0; j < 32; ++j) {
    if (j + 1 < 32) {
      if (((j + 1) & 1) == 0) {
        const int m = (j + 1) >> 1;
        asm volatile("s_waitcnt vmcnt(%2)\n\ts_barrier" : "+v"(h[2 * m]), "+v"(h[2 * m + 1]) : "i"(45 - 3 * m) : "memory");
      }
#pragma unroll
      for (int n = 0; n < 4; ++n) Wf[(j + 1) & 1][n] = tr_frag(wa[n] + 4096 * (j + 1), wa[n] + 4096 * (j + 1) + 512);
    }
    const v8bf hb = __builtin_bit_cast(v8bf, h[j]);
#pragma unroll
    for (int n = 0; n < 4; ++n) acc[n] = __builtin_amdgcn_mfma_f32_32x32x16_bf16(Wf[j & 1][n], hb, acc[n], 0, 0, 0);
#pragma unroll
    for (int i = 0; i < 4; ++i) { s1 = dot2(h[j][i], 0x3f803f80u, s1); s2 = dot2(h[j][i], h[j][i], s2); }
    __builtin_amdgcn_sched_barrier(0);
  }
  s1 += __shfl_xor(s1, 32);
  s2 += __shfl_xor(s2, 32);
  const float mu = s1 * (1.f / D);
  const float rs = rsqrtf(fmaxf(s2 * (1.f / D) - mu * mu, 0.f) + LN_EPS);
  const float nm = -mu * rs;
  v4u* act = (v4u*)(ws + WS_ACT) + (size_t)(8 * sl + hh) * 2048 + srow;
#pragma unroll
  for (int n = 0; n < 4; ++n) {
    const u32 ca = sb + K2_CS + (16 * n + 4 * hh) * 4;
    const f4 cg0 = ldsf4(ca), cg1 = ldsf4(ca + 32), cu0 = ldsf4(ca + 256), cu1 = ldsf4(ca + 256 + 32);
    float a[8];
#pragma unroll
    for (int i = 0; i < 8; ++i) {
      const float gv = fmaf(rs, acc[n][i], nm * (i < 4 ? cg0[i & 3] : cg1[i & 3]));
      const float uv = fmaf(rs, acc[n][8 + i], nm * (i < 4 ? cu0[i & 3] : cu1[i & 3]));
      const float ex = __builtin_amdgcn_exp2f(gv * -1.44269504088896341f);
      a[i] = gv * uv * __builtin_amdgcn_rcpf(1.f + ex);
    }
    act[(size_t)(2 * n) * 2048] = v4u{pk2(a[0], a[1]), pk2(a[2], a[3]), pk2(a[4], a[5]), pk2(a[6], a[7])};
  }
  STAMP(2, 1);
}

constexpr int K3_LDS = 131072;

__global__ __launch_bounds__(256, 1) void k3_gemm2(const u16* __restrict__ wd, char* __restrict__ ws, u16* __restrict__ out STAMP_ARG) {
  __shared__ __attribute__((aligned(256))) char sm[K3_LDS];
  STAMP(3, 0);
  const int bid = blockIdx.x, tid = threadIdx.x, lane = tid & 63;
  const int w = __builtin_amdgcn_readfirstlane(tid >> 6);
  const int xcd = bid & 7, idx = bid >> 3;
  int rt, ct;
  if (MAP3 == 0) { rt = xcd * 4 + (idx >> 3); ct = idx & 7; }
  else { rt = (xcd >> 1) * 8 + (idx >> 2); ct = (xcd & 1) * 4 + (idx & 3); }
  const u32 sb = (u32)(uintptr_t)(LAS char*)sm;
  const u32 wb = sb + w * 32768;
  const v4u srdW = mk_srd(ws, 0x7fffffffu);
  const v4u srdD = mk_srd(wd, (u32)D * F * 2);
  const int hh = lane >> 5, s31 = lane & 31;
  const int qa = w >> 1, qb = w & 1;
  v4u hres[2];
  {
    const u32 vo = ((ct * 8 + qa * 4 + hh) * 2048 + rt * 64 + qb * 32 + s31) * 16;
    GLD128(hres[0], srdW, vo, (u32)WS_HF);
    GLD128(hres[1], srdW, vo, (u32)WS_HF + 2 * 32768);
  }
  const u32 dvo = (lane >> 3) * 1024 + ct * 128 + (((((lane & 7) >> 2) ^ ((lane >> 4) & 1)) << 2) | (lane & 3)) * 16;
  const u32 avo = (hh * 2048 + rt * 64 + s31) * 16;
  const u32 J0 = 28 * w;
  v4u a[28][2];
#define K3_ISSUE(i) do { \
    DMA16(wb + ((i) & 15) * 2048, srdD, dvo, (J0 + (i)) * 16384, 0); \
    DMA16(wb + ((i) & 15) * 2048, srdD, dvo, (J0 + (i)) * 16384 + 7168, 1024); \
    GLD128(a[i][0], srdW, avo, (u32)WS_ACT + (J0 + (i)) * 65536); \
    GLD128O(a[i][1], srdW, avo, (u32)WS_ACT + (J0 + (i)) * 65536, 512); } while (0)
#pragma unroll
  for (int i = 0; i < 15; ++i) K3_ISSUE(i);
  u32 wa[2];
  {
    const int blk = (lane >> 4) & 1, q = (lane & 15) >> 2, pp = lane & 3;
#pragma unroll
    for (int t = 0; t < 2; ++t) wa[t] = wb + 128 * (4 * hh + q) + 64 * (t ^ ((q >> 1) & 1)) + 32 * blk + 8 * pp;
  }
  f16v acc[2][2];
#pragma unroll
  for (int t = 0; t < 4; ++t)
#pragma unroll
    for (int i = 0; i < 16; ++i) acc[t >> 1][t & 1][i] = 0.f;
  asm volatile("s_waitcnt vmcnt(56)" : "+v"(a[0][0]), "+v"(a[0][1]), "+v"(hres[0]), "+v"(hres[1]) :: "memory");
  v8bf Wf[2][2];
#pragma unroll
  for (int t = 0; t < 2; ++t) Wf[0][t] = tr_frag(wa[t], wa[t] + 1024);
#pragma unroll
  for (int i = 0; i < 28; ++i) {
    if (i + 1 < 28) {
      const int last = (i + 14 < 27) ? i + 14 : 27;
      asm volatile("s_waitcnt vmcnt(%2)" : "+v"(a[i + 1][0]), "+v"(a[i + 1][1]) : "i"(4 * (last - (i + 1))) : "memory");
#pragma unroll
      for (int t = 0; t < 2; ++t) Wf[(i + 1) & 1][t] = tr_frag(wa[t] + ((i + 1) & 15) * 2048, wa[t] + ((i + 1) & 15) * 2048 + 1024);
      if (i + 15 < 28) K3_ISSUE(i + 15);
    }
#pragma unroll
    for (int t = 0; t < 2; ++t)
#pragma unroll
      for (int b = 0; b < 2; ++b)
        acc[t][b] = __builtin_amdgcn_mfma_f32_32x32x16_bf16(Wf[i & 1][t], __builtin_bit_cast(v8bf, a[i][b]), acc[t][b], 0, 0, 0);
    __builtin_amdgcn_sched_barrier(0);
  }
  __syncthreads();
#pragma unroll
  for (int c = 0; c < 4; ++c) {
    if (c != w) {
      const int slot = (w - c - 1) & 3;
      const u32 dst = sb + (c * 3 + slot) * 4096 + lane * 16;
#pragma unroll
      for (int r = 0; r < 4; ++r) {
        const f16v& v = acc[c >> 1][c & 1];
        stsf4(dst + r * 1024, f4{v[4 * r], v[4 * r + 1], v[4 * r + 2], v[4 * r + 3]});
      }
    }
  }
  f16v mine = (w == 0) ? acc[0][0] : (w == 1) ? acc[0][1] : (w == 2) ? acc[1][0] : acc[1][1];
  __syncthreads();
#pragma unroll
  for (int sl = 0; sl < 3; ++sl)
#pragma unroll
    for (int r = 0; r < 4; ++r) {
      const f4 t = ldsf4(sb + (w * 3 + sl) * 4096 + r * 1024 + lane * 16);
#pragma unroll
      for (int i = 0; i < 4; ++i) mine[4 * r + i] += t[i];
    }
  u16* orow = out + (size_t)(rt * 64 + qb * 32 + s31) * D + ct * 64 + qa * 32 + hh * 8;
#pragma unroll
  for (int kp = 0; kp < 2; ++kp) {
    float o[8];
#pragma unroll
    for (int r = 0; r < 4; ++r) {
      auto sw = __builtin_amdgcn_permlane32_swap(__float_as_uint(mine[8 * kp + r]), __float_as_uint(mine[8 * kp + 4 + r]), false, false);
      o[r] = __uint_as_float(sw[0]);
      o[4 + r] = __uint_as_float(sw[1]);
    }
    v4u res;
#pragma unroll
    for (int r = 0; r < 4; ++r) res[r] = pk2(o[2 * r] + blo(hres[kp][r]), o[2 * r + 1] + bhi(hres[kp][r]));
    *(v4u*)(orow + kp * 16) = res;
  }
  STAMP(3, 1);
}

extern "C" void kernel_launch(void* const* d_in, const int* in_sizes, int n_in, void* d_out, int out_size, void* d_ws,
                              size_t ws_size, hipStream_t stream) {
  const u16* q = (const u16*)d_in[0];
  const u16* k = (const u16*)d_in[1];
  const u16* v = (const u16*)d_in[2];
  const u16* shift_k = (const u16*)d_in[3];
  const u16* ssm_k = (const u16*)d_in[4];
  const u16* ssm_D = (const u16*)d_in[5];
  const u16* wg = (const u16*)d_in[7];
  const u16* wu = (const u16*)d_in[8];
  const u16* wd = (const u16*)d_in[9];
  char* ws = (char*)d_ws;
  k0_prep<<<256, 256, 0, stream>>>(q, k, v, shift_k, ssm_k, ssm_D, wg, wu, wd, ws STAMP_PASS);
  k1_conv<<<256, 256, 0, stream>>>(ssm_k, ws STAMP_PASS);
  k2_gemm1<<<224, 512, 0, stream>>>(wg, wu, ws STAMP_PASS);
  k3_gemm2<<<256, 256, 0, stream>>>(wd, ws, (u16*)d_out STAMP_PASS);
}
```

```cpp
#include <hip/hip_runtime.h>
#include <stdint.h>

typedef unsigned short u16;
typedef unsigned u32;
typedef unsigned long long u64;
typedef u32 v4u __attribute__((ext_vector_type(4)));
typedef u32 v2u __attribute__((ext_vector_type(2)));
typedef float f4 __attribute__((ext_vector_type(4)));
typedef __bf16 v8bf __attribute__((ext_vector_type(8)));
typedef __bf16 v2bf __attribute__((ext_vector_type(2)));
typedef short v4s __attribute__((ext_vector_type(4)));
typedef short v8s __attribute__((ext_vector_type(8)));
#define LAS __attribute__((address_space(3)))
#define GAS __attribute__((address_space(1)))
#define DEVI __device__ __forceinline__

constexpr int S = 2048, D = 512, F = 1792;
constexpr float LN_EPS = 1e-5f;
constexpr int SPIN_CAP = 1 << 17;

typedef float f16v __attribute__((ext_vector_type(16)));
constexpr u32 WS_EPOCH = 0;
constexpr u32 WS_F1 = 1024;
constexpr u32 WS_FH = 4096;
constexpr u32 WS_F3 = 20480;
constexpr u32 WS_XST = 32768;
constexpr u32 WS_HST = 65536;
constexpr u32 WS_UX = 131072;
constexpr u32 WS_HF = WS_UX + (4u << 20);
constexpr u32 WS_ACT = WS_HF + (2u << 20);
constexpr u32 WS_END = WS_ACT + 64u * 114688u;
constexpr size_t WS_STAMP = 16u << 20;

constexpr u32 L_W1 = 0;
constexpr u32 L_K = 131072;
constexpr u32 L_U = L_K + 8320;
constexpr u32 L_T = L_U;
constexpr u32 L_X = L_U + 9216;
constexpr u32 L_Y = L_U + 2 * 9472;
constexpr u32 L_CS = L_Y + 4096;
constexpr u32 L_EDGE = L_CS + 512;
constexpr u32 L_CNT = L_EDGE + 320;
constexpr u32 L_TOTAL = 163840;
static_assert(L_CNT + 80 <= L_TOTAL, "lds");
constexpr u32 C_1 = 0, C_2 = 4, C_R1 = 8, C_3 = 12, C_4 = 16, C_4B = 20, C_5 = 24, C_6 = 28, C_CS = 32, C_HA = 36, C_HB = 40,
              C_RA = 44, C_RB = 48, C_ACTA = 52, C_ACTB = 56, C_R3A = 60, C_R3B = 64, C_SA = 68, C_SB = 72;

#define ST(i) do {} while (0)
#define STC(i) do {} while (0)
#define STAMP(k, e) do {} while (0)
#define STAMP_ARG
#define STAMP_PASS

DEVI float blo(u32 v) { return __uint_as_float(v << 16); }
DEVI float bhi(u32 v) { return __uint_as_float(v & 0xffff0000u); }
DEVI u32 pk2(float a, float b) { v2bf p = {(__bf16)a, (__bf16)b}; return __builtin_bit_cast(u32, p); }
DEVI v4u mk_srd(const void* p, u32 bytes) {
  u64 a = (u64)p;
  v4u r = {(u32)a, (u32)(a >> 32) & 0xffffu, bytes, 0x00020000u};
  return r;
}
#define GLD128(dst, srd, voff, soff, imm) \
  asm volatile("buffer_load_dwordx4 %0, %1, %2, %3 offen offset:%4" : "=v"(dst) : "v"(voff), "s"(srd), "s"(soff), "i"(imm) : "memory")
#define GLD128C(dst, srd, voff, soff, imm) \
  asm volatile("buffer_load_dwordx4 %0, %1, %2, %3 offen offset:%4 sc1" : "=v"(dst) : "v"(voff), "s"(srd), "s"(soff), "i"(imm) : "memory")
#define GLD64C(dst, srd, voff, soff, imm) \
  asm volatile("buffer_load_dwordx2 %0, %1, %2, %3 offen offset:%4 sc1" : "=v"(dst) : "v"(voff), "s"(srd), "s"(soff), "i"(imm) : "memory")
#define GLD32(dst, srd, voff, soff, imm) \
  asm volatile("buffer_load_dword %0, %1, %2, %3 offen offset:%4" : "=v"(dst) : "v"(voff), "s"(srd), "s"(soff), "i"(imm) : "memory")
#define GLD32C(dst, srd, voff, soff, imm) \
  asm volatile("buffer_load_dword %0, %1, %2, %3 offen offset:%4 sc1" : "=v"(dst) : "v"(voff), "s"(srd), "s"(soff), "i"(imm) : "memory")
#define GST128C(src, srd, voff, soff, imm) \
  asm volatile("buffer_store_dwordx4 %0, %1, %2, %3 offen offset:%4 sc1\n\ts_nop 1" :: "v"(src), "v"(voff), "s"(srd), "s"(soff), "i"(imm) : "memory")
#define GST32C(src, srd, voff, soff, imm) \
  asm volatile("buffer_store_dword %0, %1, %2, %3 offen offset:%4 sc1" :: "v"(src), "v"(voff), "s"(srd), "s"(soff), "i"(imm) : "memory")
#define DMA16(ldsaddr, srd, voff, soff, imm) \
  asm volatile("s_mov_b32 m0, %0\n\ts_nop 0\n\tbuffer_load_dwordx4 %1, %2, %3 offen offset:%4 lds" \
               :: "s"(ldsaddr), "v"(voff), "s"(srd), "s"(soff), "i"(imm) : "memory")
#define DMA16G(ldsaddr, gptr) \
  asm volatile("s_mov_b32 m0, %0\n\ts_nop 0\n\tglobal_load_lds_dwordx4 %1, off" :: "s"(ldsaddr), "v"(gptr) : "memory")
#define WAITV(n) asm volatile("s_waitcnt vmcnt(%0)" :: "i"(n) : "memory")
#define BAR() asm volatile("s_waitcnt lgkmcnt(0)\n\ts_barrier" ::: "memory")

DEVI v8bf tr_frag(u32 a0, u32 a1) {
  v4s lo = __builtin_amdgcn_ds_read_tr16_b64_v4i16((LAS v4s*)(uintptr_t)a0);
  v4s hi = __builtin_amdgcn_ds_read_tr16_b64_v4i16((LAS v4s*)(uintptr_t)a1);
  v8s r = __builtin_shufflevector(lo, hi, 0, 1, 2, 3, 4, 5, 6, 7);
  return __builtin_bit_cast(v8bf, r);
}
DEVI v4u lds128(u32 a) { return *(LAS v4u*)(uintptr_t)a; }
DEVI f4 ldsf4(u32 a) { return *(LAS f4*)(uintptr_t)a; }
DEVI void sts128(u32 a, v4u v) { *(LAS v4u*)(uintptr_t)a = v; }
DEVI void sts64(u32 a, v2u v) { *(LAS v2u*)(uintptr_t)a = v; }
DEVI void sts32(u32 a, u32 v) { *(LAS u32*)(uintptr_t)a = v; }
DEVI void stsf4(u32 a, f4 v) { *(LAS f4*)(uintptr_t)a = v; }
DEVI f4 mfma16(v8bf a, v8bf b, f4 c) { return __builtin_amdgcn_mfma_f32_16x16x32_bf16(a, b, c, 0, 0, 0); }

template <int CTRL> DEVI float dpp_add(float v) {
  return v + __uint_as_float(__builtin_amdgcn_update_dpp(0u, __float_as_uint(v), CTRL, 0xf, 0xf, true));
}
DEVI float wave_sum(float v) {
  v = dpp_add<0xb1>(v);
  v = dpp_add<0x4e>(v);
  v = dpp_add<0x141>(v);
  v = dpp_add<0x140>(v);
  auto a = __builtin_amdgcn_permlane16_swap(__float_as_uint(v), __float_as_uint(v), false, false);
  v = __uint_as_float(a[0]) + __uint_as_float(a[1]);
  auto b = __builtin_amdgcn_permlane32_swap(__float_as_uint(v), __float_as_uint(v), false, false);
  return __uint_as_float(b[0]) + __uint_as_float(b[1]);
}
DEVI v4u row_granule(v4u x, u32 tag) {
  float e[8] = {blo(x.x), bhi(x.x), blo(x.y), bhi(x.y), blo(x.z), bhi(x.z), blo(x.w), bhi(x.w)};
  const float mu = wave_sum(((e[0] + e[1]) + (e[2] + e[3])) + ((e[4] + e[5]) + (e[6] + e[7]))) * (1.f / D);
  float q = 0.f;
#pragma unroll
  for (int j = 0; j < 8; ++j) { const float t = e[j] - mu; q = fmaf(t, t, q); }
  const float rs = rsqrtf(wave_sum(q) * (1.f / D) + LN_EPS);
  return v4u{__float_as_uint(mu), tag, __float_as_uint(rs), ~tag};
}
DEVI bool gran_ok(v4u g, u32 tag) { return g.y == tag && g.w == ~tag; }

DEVI void lds_spin(u32 addr, u32 want) {
  for (int sp = 0; sp < SPIN_CAP * 4; ++sp) {
    if (*(volatile LAS u32*)(uintptr_t)addr >= want) break;
    __builtin_amdgcn_s_sleep(1);
  }
  asm volatile("" ::: "memory");
}
DEVI u32 lds_add(u32 addr) {
  u32 old = 0;
  if ((threadIdx.x & 63) == 0) old = __hip_atomic_fetch_add((LAS u32*)(uintptr_t)addr, 1u, __ATOMIC_RELAXED, __HIP_MEMORY_SCOPE_WORKGROUP);
  return __builtin_amdgcn_readfirstlane(old);
}
DEVI void poll32(v4u srd, u32 voff, u32 soff, u32 tag) {
  for (int sp = 0; sp < SPIN_CAP; ++sp) {
    u32 f;
    asm volatile("buffer_load_dword %0, %1, %2, %3 offen sc1\n\ts_waitcnt vmcnt(0)" : "=v"(f) : "v"(voff), "s"(srd), "s"(soff) : "memory");
    if (__all(f == tag)) break;
    __builtin_amdgcn_s_sleep(1);
  }
}
DEVI void poll256(v4u srd, u32 voff, u32 soff, u32 tag) {
  for (int sp = 0; sp < SPIN_CAP; ++sp) {
    v4u f;
    asm volatile("buffer_load_dwordx4 %0, %1, %2, %3 offen sc1\n\ts_waitcnt vmcnt(0)" : "=v"(f) : "v"(voff), "s"(srd), "s"(soff) : "memory");
    if (__all(f.x == tag && f.y == tag && f.z == tag && f.w == tag)) break;
    __builtin_amdgcn_s_sleep(1);
  }
}

DEVI void lds_signal(u32 addr) {
  asm volatile("s_waitcnt lgkmcnt(0)" ::: "memory");
  if ((threadIdx.x & 63) == 0) __hip_atomic_fetch_add((LAS u32*)(uintptr_t)addr, 1u, __ATOMIC_RELAXED, __HIP_MEMORY_SCOPE_WORKGROUP);
  asm volatile("" ::: "memory");
}
DEVI void lds_wait(u32 addr, u32 want) {
  for (int sp = 0; sp < SPIN_CAP * 16; ++sp)
    if (*(volatile LAS u32*)(uintptr_t)addr >= want) break;
  asm volatile("" ::: "memory");
}
DEVI void row_stats(v4u x, float& mu, float& rs) {
  float e[8] = {blo(x.x), bhi(x.x), blo(x.y), bhi(x.y), blo(x.z), bhi(x.z), blo(x.w), bhi(x.w)};
  mu = wave_sum(((e[0] + e[1]) + (e[2] + e[3])) + ((e[4] + e[5]) + (e[6] + e[7]))) * (1.f / D);
  float q = 0.f;
#pragma unroll
  for (int j = 0; j < 8; ++j) { const float t = e[j] - mu; q = fmaf(t, t, q); }
  rs = rsqrtf(wave_sum(q) * (1.f / D) + LN_EPS);
}
#define GLD64(dst, srd, voff, soff, imm) \
  asm volatile("buffer_load_dwordx2 %0, %1, %2, %3 offen offset:%4" : "=v"(dst) : "v"(voff), "s"(srd), "s"(soff), "i"(imm) : "memory")
#define SB() __builtin_amdgcn_sched_barrier(0)
template <int I> struct IC { static constexpr int v = I; };
template <int I, int N, typename FN> DEVI void static_for(FN&& fn) {
  if constexpr (I < N) { fn(IC<I>{}); static_for<I + 1, N>(fn); }
}

DEVI void conv_out(const f4 (&acc)[4], const u32 (&uv)[4], float dch, u32 yaddr, u32 eaddr, u32 bsrc, int lane) {
  const float c0 = acc[1][3] + acc[2][2] + acc[3][1], c1 = acc[2][3] + acc[3][2], c2 = acc[3][3];
  float i0 = __uint_as_float(__builtin_amdgcn_ds_bpermute(bsrc, __float_as_uint(c0)));
  float i1 = __uint_as_float(__builtin_amdgcn_ds_bpermute(bsrc, __float_as_uint(c1)));
  float i2 = __uint_as_float(__builtin_amdgcn_ds_bpermute(bsrc, __float_as_uint(c2)));
  if (lane == 63) stsf4(eaddr, f4{c0, c1, c2, 0.f});
  const float o0 = fmaf(blo(uv[0]), dch, acc[0][0]);
  const float o1 = fmaf(blo(uv[1]), dch, acc[0][1] + acc[1][0]);
  const float o2 = fmaf(blo(uv[2]), dch, acc[0][2] + acc[1][1]) + acc[2][0];
  const float o3 = fmaf(blo(uv[3]), dch, acc[0][3] + acc[1][2]) + (acc[2][1] + acc[3][0]);
  if (lane == 0) { i0 = 0.f; i1 = 0.f; i2 = 0.f; }
  sts64(yaddr, v2u{pk2(o0 + i0, o1 + i1), pk2(o2 + i2, o3)});
}
template <int T0, int T1, int T2, int T3, typename STA>
DEVI void conv_wave(u32 abase, u32 ubase, u32 uval, float dch, u32 ybase, u32 ebase, u32 bsrc, int lane, u32 cb, STA stamp) {
  constexpr int PF = 2;
  v8bf A[2 * T3 + 2];
  v8bf B[2 * T3 + 2][4];
  auto ldA = [&](int i) { A[i] = tr_frag(abase + 256 * i + 32, abase + 256 * i); };
  auto ldB = [&](int dl, int r) { B[dl + 1][r] = __builtin_bit_cast(v8bf, lds128(ubase + r * 1152 + 64 * (dl + 1))); };
  f4 acc[2][4];
  u32 uv[2][4];
#pragma unroll
  for (int t = 0; t < 2; ++t)
#pragma unroll
    for (int r = 0; r < 4; ++r) acc[t][r] = f4{0.f, 0.f, 0.f, 0.f};
#pragma unroll
  for (int st = 0; st < PF; ++st) {
    ldA(st);
#pragma unroll
    for (int r = 0; r < 4; ++r) ldB(2 * T1 - st, r);
  }
#pragma unroll
  for (int st = 0; st < 2 * T1 + 2; ++st) {
    const int dl = 2 * T1 - st;
#pragma unroll
    for (int r = 0; r < 4; ++r) {
      if (st + PF < 2 * T1 + 2) {
        if (r == 0) ldA(st + PF);
        ldB(dl - PF, r);
      } else {
        uv[st + PF - 2 * T1 - 2][r] = *(LAS u16*)(uintptr_t)(uval + r * 1152 + (st + PF == 2 * T1 + 2 ? T0 : T1) * 128);
      }
      SB();
      acc[1][r] = mfma16(A[st], B[dl + 1][r], acc[1][r]);
      if (dl <= 2 * T0) acc[0][r] = mfma16(A[2 * T0 - dl], B[dl + 1][r], acc[0][r]);
      SB();
    }
  }
#pragma unroll
  for (int st = 0; st < PF; ++st)
#pragma unroll
    for (int r = 0; r < 4; ++r) ldB(2 * T3 - st, r);
  SB();
  conv_out(acc[0], uv[0], dch, ybase + T0 * 512, ebase + (T0 + 1) * 16, bsrc, lane);
  conv_out(acc[1], uv[1], dch, ybase + T1 * 512, ebase + (T1 + 1) * 16, bsrc, lane);
  lds_signal(cb + C_4);
  stamp();
#pragma unroll
  for (int t = 0; t < 2; ++t)
#pragma unroll
    for (int r = 0; r < 4; ++r) acc[t][r] = f4{0.f, 0.f, 0.f, 0.f};
#pragma unroll
  for (int st = 0; st < 2 * T3 + 2; ++st) {
    const int dl = 2 * T3 - st;
#pragma unroll
    for (int r = 0; r < 4; ++r) {
      if (r == 0 && st + PF < 2 * T3 + 2 && st + PF >= 2 * T1 + 2) ldA(st + PF);
      if (dl - PF > 2 * T1) ldB(dl - PF, r);
      if (st >= 2 * T3) uv[st - 2 * T3][r] = *(LAS u16*)(uintptr_t)(uval + r * 1152 + (st == 2 * T3 ? T2 : T3) * 128);
      SB();
      acc[1][r] = mfma16(A[st], B[dl + 1][r], acc[1][r]);
      if (dl <= 2 * T2) acc[0][r] = mfma16(A[2 * T2 - dl], B[dl + 1][r], acc[0][r]);
      SB();
    }
  }
#pragma unroll
  for (int t = 0; t < 2; ++t) asm volatile("" : "+v"(acc[t][0]), "+v"(acc[t][1]), "+v"(acc[t][2]), "+v"(acc[t][3]));
  lds_wait(cb + C_4B, 4);
  conv_out(acc[0], uv[0], dch, ybase + (T2 & 3) * 512, ebase + (T2 + 1) * 16, bsrc, lane);
  conv_out(acc[1], uv[1], dch, ybase + (T3 & 3) * 512, ebase + (T3 + 1) * 16, bsrc, lane);
}

#define G1_WIN 8
#define W1_FIRST 8
constexpr int W2_J0 = 18;
constexpr int g1_w2(int j) { return (j >= W2_J0 && j < W2_J0 + 14) ? 2 : 0; }
constexpr int g1_ops(int j) { return (j + G1_WIN < 32 ? 1 : 0) + g1_w2(j) + (j == 12 ? 2 : 0) + (j == 26 ? 1 : 0); }
constexpr int g1_issued(int j) { int n = G1_WIN; for (int i = 0; i < j; ++i) n += g1_ops(i); return n; }
constexpr int g1_hidx(int j) { return j < G1_WIN ? j + 1 : g1_issued(j - G1_WIN) + 1; }
constexpr int g1_wait(int j) { return g1_issued(j) - g1_hidx(j); }
constexpr int g1_tail() { return g1_issued(32) - g1_issued(27); }

DEVI float silu_mul(float g, float u) {
  return g * u * __builtin_amdgcn_rcpf(1.f + __builtin_amdgcn_exp2f(g * -1.44269504088896341f));
}

__global__ __launch_bounds__(512, 2) void h3_block(const u16* __restrict__ x, const u16* __restrict__ kin, const u16* __restrict__ vin,
                                                   const u16* __restrict__ shift_k, const u16* __restrict__ ssm_k,
                                                   const u16* __restrict__ ssm_D, const u16* __restrict__ wg,
                                                   const u16* __restrict__ wu, const u16* __restrict__ wd, char* ws,
                                                   u16* __restrict__ out STAMP_ARG) {
  __shared__ __attribute__((aligned(256))) char sm[L_TOTAL];
  STAMP(0, 0);
  const int bid = blockIdx.x, tid = threadIdx.x, lane = tid & 63;
  {
    const u32 s0 = (u32)(uintptr_t)(LAS char*)sm;
    const v4u z = {0, 0, 0, 0};
    if (tid < 11) sts128(s0 + (tid < 5 ? L_CNT + tid * 16 : tid < 9 ? L_K + ((tid - 5) >> 1) * 4128 + ((tid - 5) & 1) * 16 : L_EDGE + (tid - 9) * 144), z);
  }
  asm volatile("s_waitcnt lgkmcnt(0)\n\ts_barrier" :: "s"(x), "s"(kin), "s"(vin), "s"(shift_k), "s"(ssm_k), "s"(ssm_D), "s"(wg), "s"(wu), "s"(wd), "s"(ws), "s"(out) : "memory");
  const int w = __builtin_amdgcn_readfirstlane(tid >> 6);
  const int xcd = bid & 7, idx = bid >> 3;
  ST(0); STC(24);
  const u32 sb = (u32)(uintptr_t)(LAS char*)sm;
  const u32 cb = sb + L_CNT;
  const v4u srdW = mk_srd(ws, WS_END);
  u32 tag;
  const u32 l16 = lane * 16;
  const int n16 = lane & 15, g4 = lane >> 4;
  const int P = xcd * 32 + idx;
  const bool g1 = idx < 28;
  const int i7 = (idx * 37) >> 8;
  const int grp1 = g1 ? (xcd & 4) + i7 : xcd;
  const int sl = 7 * (xcd & 3) + (idx - 7 * i7);
  const int roth = ((idx - 7 * i7) * 9) >> 1;
  const u32 wvo = (lane >> 3) * (F * 2) + sl * 128 + ((((lane & 7) >> 1) ^ ((((lane >> 4) & 1) << 1) | (w & 1))) * 32) + (lane & 1) * 16;
  const v4u srdM = mk_srd((w & 1) ? wu : wg, g1 ? (u32)D * F * 2 : 0u);

  if (w < 4) {
    const int cq = tid & 15, rq = tid >> 4;
    const int r0 = idx * 64 + rq * 4;
    const v4u srdK = mk_srd(kin, S * D * 2), srdV = mk_srd(vin, S * D * 2), srdX = mk_srd(x, S * D * 2);
    v2u kk[7], vv[4], xx[4];
    u32 tagv;
    const u32 vo = r0 * 1024 + xcd * 128 + cq * 8;
    {
      const u32 vk = vo - 3072;
      GLD64(kk[0], srdK, vk, 0, 0); GLD64(kk[1], srdK, vk, 0, 1024); GLD64(kk[2], srdK, vk, 0, 2048);
    }
#pragma unroll
    for (int i = 0; i < 4; ++i) GLD64(kk[3 + i], srdK, vo, 0, i * 1024);
#pragma unroll
    for (int i = 0; i < 4; ++i) GLD64(vv[i], srdV, vo, 0, i * 1024);
    v4u sk0, sk1;
    {
      const v4u srdH = mk_srd(shift_k, D * 8);
      GLD128(sk0, srdH, (u32)(xcd * 512 + cq * 32), 0, 0);
      GLD128(sk1, srdH, (u32)(xcd * 512 + cq * 32), 0, 16);
    }
#pragma unroll
    for (int i = 0; i < 4; ++i) GLD64(xx[i], srdX, vo, 0, i * 1024);
    u32 dd;
    GLD32(dd, mk_srd(ssm_D, D * 2), (u32)P * 4, 0, 0);
    GLD32(tagv, srdW, 0u, WS_EPOCH, 0);
    ST(22);
    asm volatile("s_waitcnt vmcnt(6)"
                 : "+v"(kk[0]), "+v"(kk[1]), "+v"(kk[2]), "+v"(kk[3]), "+v"(kk[4]), "+v"(kk[5]), "+v"(kk[6]), "+v"(vv[0]), "+v"(vv[1]),
                   "+v"(vv[2]), "+v"(vv[3]), "+v"(sk0), "+v"(sk1) :: "memory");
    ST(1);
    if (r0 == 0) { kk[0] = v2u{0, 0}; kk[1] = v2u{0, 0}; kk[2] = v2u{0, 0}; }
    u32 uu[4][2];
#pragma unroll
    for (int c = 0; c < 2; ++c) {
      const v4u sk = c ? sk1 : sk0;
      const float wa0 = blo(sk.x), wa1 = bhi(sk.x), wa2 = blo(sk.y), wa3 = bhi(sk.y);
      const float wb0 = blo(sk.z), wb1 = bhi(sk.z), wb2 = blo(sk.w), wb3 = bhi(sk.w);
#pragma unroll
      for (int i = 0; i < 4; ++i) {
        float a = blo(kk[i][c]) * wa3; a = fmaf(blo(kk[i + 1][c]), wa2, a); a = fmaf(blo(kk[i + 2][c]), wa1, a); a = fmaf(blo(kk[i + 3][c]), wa0, a);
        float b = bhi(kk[i][c]) * wb3; b = fmaf(bhi(kk[i + 1][c]), wb2, b); b = fmaf(bhi(kk[i + 2][c]), wb1, b); b = fmaf(bhi(kk[i + 3][c]), wb0, b);
        uu[i][c] = pk2(a * blo(vv[i][c]), b * bhi(vv[i][c]));
      }
    }
    {
      const u32 ta = sb + L_T + ((4 * cq + 2 * (rq & 1)) * 36 + (rq >> 1)) * 4;
#pragma unroll
      for (int r = 0; r < 4; ++r) {
        auto sw = __builtin_amdgcn_permlane16_swap(uu[r][0], uu[r][1], false, false);
        sts32(ta + r * 32, __builtin_amdgcn_perm(sw[1], sw[0], 0x05040100u));
        sts32(ta + 144 + r * 32, __builtin_amdgcn_perm(sw[1], sw[0], 0x07060302u));
      }
    }
    asm volatile("s_waitcnt vmcnt(0)" : "+v"(xx[0]), "+v"(xx[1]), "+v"(xx[2]), "+v"(xx[3]), "+v"(dd), "+v"(tagv) :: "memory");
    tag = __builtin_amdgcn_readfirstlane(tagv) + 1u;
    sts128(sb + L_X + (2 * cq) * 272 + rq * 16, v4u{xx[0].x, xx[1].x, xx[2].x, xx[3].x});
    sts128(sb + L_X + (2 * cq + 1) * 272 + rq * 16, v4u{xx[0].y, xx[1].y, xx[2].y, xx[3].y});
    lds_signal(cb + C_1);
    lds_wait(cb + C_1, 4);
    ST(21);
    {
      const u32 so = WS_UX + (u32)bid * 16384;
#pragma unroll
      for (int k = 0; k < 4; ++k) {
        const int pr = 8 * k + (tid >> 5), q = tid & 31;
        const v4u a = lds128(q < 16 ? sb + L_T + (2 * pr + (q >> 3)) * 144 + (q & 7) * 16 : sb + L_X + pr * 272 + (q - 16) * 16);
        GST128C(a, srdW, (u32)tid * 16, so + k * 4096, 0);
      }
    }
    WAITV(0);
    ST(2);
    const u32 arr = lds_add(cb + C_2);
    if (arr == 3) {
      if (lane == 0) GST32C(tag, srdW, (u32)(xcd * 256 + idx * 4), WS_F1, 0);
      const v4u z = {0, 0, 0, 0};
#pragma unroll
      for (int k = 0; k < 2; ++k) {
        const int a8 = lane >> 3;
        const u32 za = sb + L_U + k * 9472 + (a8 >> 2) * 4736 + (a8 & 3) * 1152;
        sts128(za + (lane & 7) * 16, z);
        if ((lane & 7) == 0) sts128(za + 128, z);
      }
      poll32(srdW, (u32)(xcd * 256 + (lane & 31) * 4), WS_F1, tag);
      asm volatile("s_waitcnt lgkmcnt(0)" ::: "memory");
      if (lane == 0) sts32(cb + C_R1, 1u);
    } else {
      lds_wait(cb + C_R1, 1u);
    }
    ST(3);
    {
      v4u ua, ub;
      const u32 uo = (u32)(tid >> 4) * (8 * 16384) + (tid & 15) * 16;
      const u32 so = WS_UX + (u32)xcd * 16384 + idx * 512;
      GLD128C(ua, srdW, uo, so, 0);
      GLD128C(ub, srdW, uo, so + 16 * 8 * 16384, 0);
      const u32 ul = sb + L_U + ((tid >> 3) & 1) * 9472 + ((tid >> 1) & 3) * 1152 + 128 + (tid >> 4) * 32 + (tid & 1) * 16;
      asm volatile("s_waitcnt vmcnt(0)" : "+v"(ua), "+v"(ub) :: "memory");
      sts128(ul, ua);
      sts128(ul + 512, ub);
      sts64(ul + 4736 + 8, v2u{ua.x, ua.y});
      sts64(ul + 4736 + 16, v2u{ua.z, ua.w});
      sts64(ul + 4736 + 512 + 8, v2u{ub.x, ub.y});
      if (!(tid >= 240 && (tid & 1))) sts64(ul + 4736 + 512 + 16, v2u{ub.z, ub.w});
    }
    lds_signal(cb + C_3);
    const int e = w >> 1;
    const float dch = e ? bhi(dd) : blo(dd);
    const u32 abase = sb + L_K + e * 4128 + 32 + 64 * g4 + 8 * ((n16 & 3) - (n16 >> 2));
    const u32 ubase = sb + L_U + e * 9472 + ((n16 & 1) ? 120 : 4736 + 128) + 8 * n16 - 16 * g4 - 64;
    const u32 uval = sb + L_U + e * 9472 + 128 + (4 * n16 + g4) * 2;
    const u32 ybase = sb + L_Y + e * 2048 + (4 * n16 + g4) * 8;
    const u32 ebase = sb + L_EDGE + e * 144;
    const u32 bsrc = (g4 > 0 ? lane - 16 : lane + 47) * 4;
    lds_wait(cb + C_3, 8);
    ST(4);
    auto stamp5 = [&]() { ST(5); };
    if (w & 1) conv_wave<1, 2, 5, 6>(abase, ubase, uval, dch, ybase, ebase, bsrc, lane, cb, stamp5);
    else conv_wave<0, 3, 4, 7>(abase, ubase, uval, dch, ybase, ebase, bsrc, lane, cb, stamp5);
    lds_signal(cb + C_5);
    ST(6);
    lds_wait(cb + C_6, 4);
    ST(7);
    if (g1) {
      const int blk = (lane >> 4) & 1, q = n16 >> 2;
      const u32 wa = sb + L_W1 + 2048 * (lane >> 5) + 1024 * blk + 128 * q + 8 * (lane & 3) + 32 * (w ^ ((((q >> 1) & 1) << 1) | blk));
      const v8bf ones = __builtin_bit_cast(v8bf, v4u{0x3f803f80u, 0x3f803f80u, 0x3f803f80u, 0x3f803f80u});
      f16v c;
#pragma unroll
      for (int i = 0; i < 16; ++i) c[i] = 0.f;
#pragma unroll 1
      for (int jj = 0; jj < 4; ++jj) {
        v8bf fr[8];
#pragma unroll
        for (int j = 0; j < 8; ++j) fr[j] = tr_frag(wa + 32768 * jj + 4096 * j, wa + 32768 * jj + 4096 * j + 512);
#pragma unroll
        for (int j = 0; j < 8; ++j) c = __builtin_amdgcn_mfma_f32_32x32x16_bf16(fr[j], ones, c, 0, 0, 0);
      }
      if ((lane & 31) == 0) {
#pragma unroll
        for (int r = 0; r < 4; ++r) stsf4(sb + L_CS + 128 * w + 32 * r + 16 * (lane >> 5), f4{c[4 * r], c[4 * r + 1], c[4 * r + 2], c[4 * r + 3]});
      }
      lds_signal(cb + C_CS);
    }
  } else {
    const int ew = w - 4;
    v4u xr[2];
    u32 tagv;
    GLD32(tagv, srdW, 0u, WS_EPOCH, 0);
    const v4u srdX = mk_srd(x, S * D * 2);
    GLD128(xr[0], srdX, l16, (u32)(bid * 8 + ew * 2) * 1024, 0);
    GLD128(xr[1], srdX, l16, (u32)(bid * 8 + ew * 2) * 1024, 1024);
    {
      const v4u srdS = mk_srd(ssm_k, D * S * 2);
      const u32 kso = (u32)(2 * P + (ew >> 1)) * 4096 + (ew & 1) * 2048, kl = sb + L_K + (ew >> 1) * 4128 + 32 + (ew & 1) * 2048;
      DMA16(kl, srdS, l16, kso, 0); DMA16(kl, srdS, l16, kso, 1024);
    }
    u32 pf0, pf1, pf2;
#define PFM 7
    GLD32(pf0, srdW, (PFM & 1) ? (u32)(lane < 14 ? lane : 0) << 20 : 0x80000000u, 0, 0);
    GLD32(pf1, mk_srd(out, S * D * 2), (PFM & 2) ? (u32)(lane & 1) << 20 : 0x80000000u, 0, 0);
    GLD32(pf2, mk_srd(wd, D * F * 2), (PFM & 4) ? (u32)(bid * 256 + ew * 64 + lane) * 28 : 0x80000000u, 0, 0);
    ST(2);
    auto w1_piece = [&](int m) {
      const int j = (m + 8 * i7) & 31;
      DMA16(sb + L_W1 + 4096 * j + 1024 * ew, srdM, wvo, (2 * ((j + roth) & 31) + (ew >> 1)) * (8 * F * 2), 0);
    };
#pragma unroll
    for (int m = 0; m < W1_FIRST; ++m) w1_piece(m);
    ST(21);
    asm volatile("s_waitcnt vmcnt(%3)" : "+v"(xr[0]), "+v"(xr[1]), "+v"(tagv) : "i"(5 + W1_FIRST) : "memory");
    tag = __builtin_amdgcn_readfirstlane(tagv) + 1u;
    ST(22);
    {
      float m0, r0, m1, r1;
      row_stats(xr[0], m0, r0);
      row_stats(xr[1], m1, r1);
      const v4u gr = {(__float_as_uint(m0) & ~255u) | (tag & 255u), (__float_as_uint(r0) & ~255u) | ((tag >> 8) & 255u),
                      (__float_as_uint(m1) & ~255u) | ((tag >> 16) & 255u), (__float_as_uint(r1) & ~255u) | (tag >> 24)};
      const u32 go = lane == 0 ? (u32)(bid * 8 + ew * 2) * 8 : 0x80000000u;
      GST128C(gr, srdW, go, WS_XST, 0);
    }
    ST(1);
    WAITV(4 + W1_FIRST);
    lds_signal(cb + C_3);
    lds_spin(cb + C_2, 4u);
#pragma unroll
    for (int m = W1_FIRST; m < 32; ++m) w1_piece(m);
    lds_spin(cb + C_R1, 1u);
    ST(3);
    u32 xq[2][4];
    v2u st[2][4];
#pragma unroll
    for (int hf = 0; hf < 2; ++hf)
#pragma unroll
      for (int r = 0; r < 4; ++r) {
        const int zb = 16 * hf + 4 * ew + r;
        GLD32C(xq[hf][r], srdW, (u32)lane * 4, WS_UX + (u32)(zb * 8 + xcd) * 16384 + idx * 512 + 256, 0);
        GLD64C(st[hf][r], srdW, (u32)lane * 8, WS_XST + zb * 512, 0);
      }
    asm volatile("s_waitcnt vmcnt(0)"
                 : "+v"(pf0), "+v"(pf1), "+v"(pf2), "+v"(xq[0][0]), "+v"(xq[0][1]), "+v"(xq[0][2]), "+v"(xq[0][3]), "+v"(xq[1][0]),
                   "+v"(xq[1][1]), "+v"(xq[1][2]), "+v"(xq[1][3]), "+v"(st[0][0]), "+v"(st[0][1]), "+v"(st[0][2]), "+v"(st[0][3]),
                   "+v"(st[1][0]), "+v"(st[1][1]), "+v"(st[1][2]), "+v"(st[1][3]) :: "memory");
    lds_signal(cb + C_6);
    {
      const u32 t0 = (tag >> (16 * (lane & 1))) & 255u, t1 = (tag >> (16 * (lane & 1) + 8)) & 255u;
      for (int sp = 0; sp < SPIN_CAP; ++sp) {
        bool ok = true;
#pragma unroll
        for (int hf = 0; hf < 2; ++hf)
#pragma unroll
          for (int r = 0; r < 4; ++r) ok = ok && (st[hf][r].x & 255u) == t0 && (st[hf][r].y & 255u) == t1;
        if (__all(ok)) break;
        __builtin_amdgcn_s_sleep(2);
#pragma unroll
        for (int hf = 0; hf < 2; ++hf)
#pragma unroll
          for (int r = 0; r < 4; ++r) GLD64C(st[hf][r], srdW, (u32)lane * 8, WS_XST + (16 * hf + 4 * ew + r) * 512, 0);
        asm volatile("s_waitcnt vmcnt(0)"
                     : "+v"(st[0][0]), "+v"(st[0][1]), "+v"(st[0][2]), "+v"(st[0][3]), "+v"(st[1][0]), "+v"(st[1][1]), "+v"(st[1][2]),
                       "+v"(st[1][3]) :: "memory");
      }
    }
    ST(4);
    const u32 hvo = (u32)(P >> 2) * 512 + (P & 3) * 4 + (lane >> 5) * 32768 + (lane & 31) * 16;
#pragma unroll
    for (int hf = 0; hf < 2; ++hf) {
      lds_spin(cb + (hf ? C_5 : C_4), 4);
      ST(5 + 2 * hf);
      const int tile = hf * 4 + ew;
      const u32 ya = sb + L_Y + ew * 512 + lane * 2;
      const u32 ea = sb + L_EDGE + (lane < 3 ? tile * 16 + lane * 4 : 0);
      float z0[4], z1[4];
#pragma unroll
      for (int r = 0; r < 4; ++r) {
        z0[r] = blo(*(LAS u16*)(uintptr_t)(ya + r * 128));
        z1[r] = blo(*(LAS u16*)(uintptr_t)(ya + 2048 + r * 128));
      }
      z0[0] += *(LAS float*)(uintptr_t)ea;
      z1[0] += *(LAS float*)(uintptr_t)(ea + 144);
      if (hf == 0) {
#pragma unroll
        for (int r = 0; r < 4; ++r) asm volatile("" : "+v"(z0[r]), "+v"(z1[r]));
        lds_signal(cb + C_4B);
      }
#pragma unroll
      for (int r = 0; r < 4; ++r) {
        const float mu = __uint_as_float(st[hf][r].x), rs = __uint_as_float(st[hf][r].y);
        const float xa = blo(xq[hf][r]), xb = bhi(xq[hf][r]);
        const u32 hv = pk2(fmaf((xa - mu) * rs, z0[r], xa), fmaf((xb - mu) * rs, z1[r], xb));
        GST32C(hv, srdW, hvo, WS_HF + (u32)(tile * 8 + r * 2) * 32768, 0);
      }
      WAITV(0);
      if (lds_add(cb + (hf ? C_HB : C_HA)) == 3) {
        if (lane < 8) GST32C(tag, srdW, (u32)(lane * 1024 + bid * 4), WS_FH + hf * 8192, 0);
      }
      ST(6 + 2 * hf);
    }
    lds_wait(cb + C_6, 4);
  }

  const int half = w >> 2, wq = w & 3;
  const int rowA = 256 * (xcd >> 1) + 128 * (xcd & 1), rowB = 256 * (7 - (xcd >> 1)) + 128 * (xcd & 1);
  const int row0 = 256 * (half ? 7 - (grp1 >> 1) : (grp1 >> 1)) + 128 * (grp1 & 1);
  ST(9);
  if (wq == 0) {
    poll256(srdW, l16, WS_FH + half * 8192 + xcd * 1024, tag);
    if (lane == 0) sts32(cb + (half ? C_RB : C_RA), 1u);
    if (half == 1 && bid == 0 && lane == 0) GST32C(tag, srdW, 0u, WS_EPOCH, 0);
  } else {
    lds_spin(cb + (half ? C_RB : C_RA), 1u);
  }
  ST(10); STC(25);
  v4u w2[7][4];
  v2u hres[2];
  const int ct = idx & 7, rsub = idx >> 3;
  const int hh = lane >> 5;
  const v4u srdD = mk_srd(wd, D * F * 2);
  const u32 dvo = (u32)(16 * (g4 >> 1) + 4 * (g4 & 1) + 8 * (n16 >> 3)) * 1024 + ct * 128 + (n16 & 7) * 16;
  const u32 dso = (u32)w * (224 * 1024);
  const u32 rvo = (u32)(ct * 8 + 4 * (g4 & 1) + wq) * 512 + (16 * half + n16) * 16 + (g4 >> 1) * 8;
  if (g1) {
    f16v acc[4];
    v4u hg;
    {
      v4u h[32];
      const u32 hso = WS_HF + (u32)((row0 >> 5) + wq) * 32768;
#pragma unroll
      for (int j = 0; j < G1_WIN; ++j) GLD128C(h[j], srdW, l16, hso + ((j + roth) & 31) * 1024, 0);
      u32 wa[4];
      {
        const int blk = (lane >> 4) & 1, q = n16 >> 2;
        const int sw = (((q >> 1) & 1) << 1) | blk;
#pragma unroll
        for (int n = 0; n < 4; ++n) {
          wa[n] = sb + L_W1 + 2048 * hh + 1024 * blk + 128 * q + 8 * (lane & 3) + 32 * (n ^ sw);
        }
      }
      constexpr int PD = 4;
      v8bf W[128];
      auto ldw = [&](int f) -> v8bf {
        const int j = f >> 2, n = f & 3;
        if (j == 16) { wa[n] += 65536; asm volatile("" : "+v"(wa[n])); }
        const u32 b = wa[n] + 4096 * (j & 15);
        return tr_frag(b, b + 512);
      };
#pragma unroll
      for (int f = 0; f < PD; ++f) W[f] = ldw(f);
#pragma unroll
      for (int n = 0; n < 4; ++n)
#pragma unroll
        for (int i = 0; i < 16; ++i) acc[n][i] = 0.f;
      static_for<0, 128>([&](auto FI) {
          constexpr int f = FI.v, j = f >> 2, n = f & 3;
          constexpr int wt = g1_wait(j);
          if constexpr (n == 0) asm volatile("s_waitcnt vmcnt(%1)" : "+v"(h[j]) : "i"(wt) : "memory");
          if constexpr (n == 0 && j + G1_WIN < 32) GLD128C(h[j + G1_WIN], srdW, l16, hso + ((j + G1_WIN + roth) & 31) * 1024, 0);
          if constexpr (n == 1 && g1_w2(j)) GLD128(w2[(j - W2_J0) >> 1][2 * (j & 1)], srdD, dvo, dso + ((j - W2_J0) >> 1) * 32768, 2048 * (j & 1));
          if constexpr (n == 2 && g1_w2(j)) GLD128(w2[(j - W2_J0) >> 1][2 * (j & 1) + 1], srdD, dvo, dso + ((j - W2_J0) >> 1) * 32768, 2048 * (j & 1) + 1024);
          if constexpr (n == 3 && j == 12) {
            GLD64C(hres[0], srdW, rvo, WS_HF + (u32)((rowA >> 5) + rsub) * 32768, 0);
            GLD64C(hres[1], srdW, rvo, WS_HF + (u32)((rowB >> 5) + rsub) * 32768, 0);
          }
          if constexpr (n == 3 && j == 26) GLD128C(hg, srdW, (u32)(lane & 31) * 16, WS_HST + (u32)(row0 + 32 * wq) * 16, 0);
          if constexpr (f + PD < 128) W[f + PD] = ldw(f + PD);
          SB();
          acc[n] = __builtin_amdgcn_mfma_f32_32x32x16_bf16(W[f], __builtin_bit_cast(v8bf, h[j]), acc[n], 0, 0, 0);
          SB();
      });
    }
    asm volatile("" : "+v"(acc[0]), "+v"(acc[1]), "+v"(acc[2]), "+v"(acc[3]));
    ST(11); STC(26);
    constexpr int tl = g1_tail();
    asm volatile("s_waitcnt vmcnt(%1)" : "+v"(hg) : "i"(tl) : "memory");
    if (!__all(gran_ok(hg, tag))) {
      for (int sp = 0; sp < SPIN_CAP; ++sp) {
        GLD128C(hg, srdW, (u32)(lane & 31) * 16, WS_HST + (u32)(row0 + 32 * wq) * 16, 0);
        asm volatile("s_waitcnt vmcnt(0)" : "+v"(hg) :: "memory");
        if (__all(gran_ok(hg, tag))) break;
        __builtin_amdgcn_s_sleep(2);
      }
    }
    lds_wait(cb + C_CS, 4u);
    const float rs = __uint_as_float(hg.z), nm = -__uint_as_float(hg.x) * rs;
    const u32 aso = WS_ACT + (u32)((row0 >> 5) + wq) * 114688 + sl * 4096;
#pragma unroll
    for (int n = 0; n < 4; ++n) {
      const u32 ca = sb + L_CS + 128 * n + 16 * hh;
      const f4 cg0 = ldsf4(ca), cg1 = ldsf4(ca + 32), cu0 = ldsf4(ca + 64), cu1 = ldsf4(ca + 96);
      float a[8];
#pragma unroll
      for (int i = 0; i < 4; ++i) {
        a[i] = silu_mul(fmaf(rs, acc[n][i], nm * cg0[i]), fmaf(rs, acc[n][8 + i], nm * cu0[i]));
        a[4 + i] = silu_mul(fmaf(rs, acc[n][4 + i], nm * cg1[i]), fmaf(rs, acc[n][12 + i], nm * cu1[i]));
      }
      const v4u o = {pk2(a[0], a[1]), pk2(a[2], a[3]), pk2(a[4], a[5]), pk2(a[6], a[7])};
      GST128C(o, srdW, l16, aso + n * 1024, 0);
    }
    ST(12);
  } else {
    v4u sr[8];
    const int r32 = row0 + 32 * (idx - 28);
#pragma unroll
    for (int i = 0; i < 8; ++i) GLD128C(sr[i], srdW, l16, WS_HF + (u32)(r32 >> 5) * 32768 + wq * 8192 + (i >> 2) * 4096, (i & 3) * 1024);
    asm volatile("s_waitcnt vmcnt(0)" : "+v"(sr[0]), "+v"(sr[1]), "+v"(sr[2]), "+v"(sr[3]), "+v"(sr[4]), "+v"(sr[5]), "+v"(sr[6]), "+v"(sr[7]) :: "memory");
    const u32 sa = sb + half * 4096;
    float s1 = 0.f;
#pragma unroll
    for (int i = 0; i < 8; ++i)
#pragma unroll
      for (int d = 0; d < 4; ++d) s1 += blo(sr[i][d]) + bhi(sr[i][d]);
    sts32(sa + wq * 256 + lane * 4, __float_as_uint(s1));
    lds_signal(cb + (half ? C_SB : C_SA));
    lds_wait(cb + (half ? C_SB : C_SA), 4);
    float mu = 0.f;
#pragma unroll
    for (int i = 0; i < 8; ++i) mu += *(LAS float*)(uintptr_t)(sa + i * 128 + (lane & 31) * 4);
    mu *= 1.f / D;
    float s2 = 0.f;
#pragma unroll
    for (int i = 0; i < 8; ++i)
#pragma unroll
      for (int d = 0; d < 4; ++d) { const float a = blo(sr[i][d]) - mu, b = bhi(sr[i][d]) - mu; s2 = fmaf(a, a, s2); s2 = fmaf(b, b, s2); }
    sts32(sa + 1024 + wq * 256 + lane * 4, __float_as_uint(s2));
    lds_signal(cb + (half ? C_SB : C_SA));
    if (wq == 0) {
      lds_wait(cb + (half ? C_SB : C_SA), 8);
      float q = 0.f;
#pragma unroll
      for (int i = 0; i < 8; ++i) q += *(LAS float*)(uintptr_t)(sa + 1024 + i * 128 + (lane & 31) * 4);
      const v4u gr = {__float_as_uint(mu), tag, __float_as_uint(rsqrtf(q * (1.f / D) + LN_EPS)), ~tag};
      if (lane < 32) GST128C(gr, srdW, l16, WS_HST + (u32)r32 * 16, 0);
    }
    ST(11); STC(26);
    GLD64C(hres[0], srdW, rvo, WS_HF + (u32)((rowA >> 5) + rsub) * 32768, 0);
    GLD64C(hres[1], srdW, rvo, WS_HF + (u32)((rowB >> 5) + rsub) * 32768, 0);
#pragma unroll
    for (int J = 0; J < 7; ++J)
#pragma unroll
      for (int i = 0; i < 4; ++i) GLD128(w2[J][i], srdD, dvo, dso + J * 32768, 1024 * i);
    ST(12);
  }
  asm volatile("s_waitcnt vmcnt(0)"
               : "+v"(hres[0]), "+v"(hres[1]), "+v"(w2[0][0]), "+v"(w2[0][1]), "+v"(w2[0][2]), "+v"(w2[0][3]), "+v"(w2[1][0]), "+v"(w2[1][1]),
                 "+v"(w2[1][2]), "+v"(w2[1][3]), "+v"(w2[2][0]), "+v"(w2[2][1]), "+v"(w2[2][2]), "+v"(w2[2][3]), "+v"(w2[3][0]),
                 "+v"(w2[3][1]), "+v"(w2[3][2]), "+v"(w2[3][3]) :: "memory");
  asm volatile("" : "+v"(w2[4][0]), "+v"(w2[4][1]), "+v"(w2[4][2]), "+v"(w2[4][3]), "+v"(w2[5][0]), "+v"(w2[5][1]), "+v"(w2[5][2]),
                 "+v"(w2[5][3]), "+v"(w2[6][0]), "+v"(w2[6][1]), "+v"(w2[6][2]), "+v"(w2[6][3]) :: "memory");
  if (lds_add(cb + (half ? C_ACTB : C_ACTA)) == 3) {
    if (lane == 0 && g1) GST32C(tag, srdW, (u32)(grp1 * 256 + sl * 4), WS_F3 + half * 2048, 0);
  }
  ST(13);
  v8bf wf[7][4];
#pragma unroll
  for (int J = 0; J < 7; ++J) {
    u32 lo[4][2], hi[4][2];
#pragma unroll
    for (int i = 0; i < 4; ++i)
#pragma unroll
      for (int d2 = 0; d2 < 2; ++d2) {
        lo[i][d2] = __builtin_amdgcn_update_dpp(w2[J][i][d2], w2[J][i][2 + d2], 0x128, 0xf, 0xc, false);
        hi[i][d2] = __builtin_amdgcn_update_dpp(w2[J][i][2 + d2], w2[J][i][d2], 0x128, 0xf, 0x3, false);
      }
#pragma unroll
    for (int a = 0; a < 4; ++a) {
      const u32 sel = (a & 1) ? 0x07060302u : 0x05040100u;
      const v4u f = {__builtin_amdgcn_perm(lo[1][a >> 1], lo[0][a >> 1], sel), __builtin_amdgcn_perm(lo[3][a >> 1], lo[2][a >> 1], sel),
                     __builtin_amdgcn_perm(hi[1][a >> 1], hi[0][a >> 1], sel), __builtin_amdgcn_perm(hi[3][a >> 1], hi[2][a >> 1], sel)};
      wf[J][a] = __builtin_bit_cast(v8bf, f);
      asm volatile("" : "+v"(wf[J][a]));
    }
  }
  ST(14);
  const u32 avo = (u32)g4 * 512 + n16 * 16;
#pragma unroll
  for (int hf = 0; hf < 2; ++hf) {
    if (w == 0) {
      poll32(srdW, (u32)(xcd * 256 + (lane < 28 ? lane : 0) * 4), WS_F3 + hf * 2048, tag);
      if (lane == 0) sts32(cb + (hf ? C_R3B : C_R3A), 1u);
    } else {
      lds_spin(cb + (hf ? C_R3B : C_R3A), 1u);
    }
    ST(15 + 3 * hf);
    const int rw = hf ? rowB : rowA;
    const u32 aso = WS_ACT + (u32)((rw >> 5) + rsub) * 114688 + w * 14336;
    v4u av[7][2];
#pragma unroll
    for (int J = 0; J < 7; ++J) {
      GLD128C(av[J][0], srdW, avo, aso + J * 2048, 0);
      GLD128C(av[J][1], srdW, avo, aso + J * 2048, 256);
    }
    f4 o2[2][4];
#pragma unroll
    for (int t = 0; t < 2; ++t)
#pragma unroll
      for (int a = 0; a < 4; ++a) o2[t][a] = f4{0.f, 0.f, 0.f, 0.f};
#pragma unroll
    for (int J = 0; J < 7; ++J) {
      asm volatile("s_waitcnt vmcnt(%2)" : "+v"(av[J][0]), "+v"(av[J][1]) : "i"(2 * (6 - J)) : "memory");
#pragma unroll
      for (int t = 0; t < 2; ++t)
#pragma unroll
        for (int a = 0; a < 4; ++a) o2[t][a] = mfma16(wf[J][a], __builtin_bit_cast(v8bf, av[J][t]), o2[t][a]);
    }
#pragma unroll
    for (int t = 0; t < 2; ++t) asm volatile("" : "+v"(o2[t][0]), "+v"(o2[t][1]), "+v"(o2[t][2]), "+v"(o2[t][3]));
    ST(16 + 3 * hf);
    if (hf == 0) lds_wait(cb + C_ACTB, 4u);
    const u32 pb = sb + hf * 65536;
#pragma unroll
    for (int t = 0; t < 2; ++t)
#pragma unroll
      for (int r = 0; r < 4; ++r) stsf4(pb + (t * 4 + r) * 8192 + w * 1024 + l16, f4{o2[t][0][r], o2[t][1][r], o2[t][2][r], o2[t][3][r]});
    BAR();
    f4 sum = ldsf4(pb + w * 8192 + l16);
#pragma unroll
    for (int i = 1; i < 8; ++i) sum += ldsf4(pb + w * 8192 + i * 1024 + l16);
    const v2u res = {pk2(sum[0] + blo(hres[hf].x), sum[1] + bhi(hres[hf].x)), pk2(sum[2] + blo(hres[hf].y), sum[3] + bhi(hres[hf].y))};
    *(v2u*)(out + (size_t)(rw + 32 * rsub + 16 * half + n16) * D + ct * 64 + 32 * (g4 & 1) + 8 * wq + 4 * (g4 >> 1)) = res;
    ST(17 + 3 * hf); if (hf) STC(27);
  }
  STAMP(0, 1);
}

extern "C" void kernel_launch(void* const* d_in, const int* in_sizes, int n_in, void* d_out, int out_size, void* d_ws,
                              size_t ws_size, hipStream_t stream) {
  char* ws = (char*)d_ws;
  h3_block<<<256, 512, 0, stream>>>((const u16*)d_in[0], (const u16*)d_in[1], (const u16*)d_in[2], (const u16*)d_in[3],
                                    (const u16*)d_in[4], (const u16*)d_in[5], (const u16*)d_in[7], (const u16*)d_in[8],
                                    (const u16*)d_in[9], ws, (u16*)d_out STAMP_PASS);
}
```

```cpp
#include <hip/hip_runtime.h>
#include <stdint.h>

typedef unsigned short u16;
typedef unsigned u32;
typedef unsigned long long u64;
typedef u32 v4u __attribute__((ext_vector_type(4)));
typedef u32 v2u __attribute__((ext_vector_type(2)));
typedef float f4 __attribute__((ext_vector_type(4)));
typedef __bf16 v8bf __attribute__((ext_vector_type(8)));
typedef __bf16 v2bf __attribute__((ext_vector_type(2)));
typedef short v4s __attribute__((ext_vector_type(4)));
typedef short v8s __attribute__((ext_vector_type(8)));
#define LAS __attribute__((address_space(3)))
#define GAS __attribute__((address_space(1)))
#if defined(__HIP_DEVICE_COMPILE__)
#define NOPK __attribute__((target("no-packed-fp32-ops")))
#else
#define NOPK
#endif
#define DEVI NOPK __device__ __forceinline__
#define u2f(x) __builtin_bit_cast(float, (u32)(x))
#define f2u(x) __builtin_bit_cast(u32, (float)(x))
#define ALL(p) (__builtin_amdgcn_ballot_w64(!(p)) == 0)
#define TID ((int)__builtin_amdgcn_workitem_id_x())
#define BID ((int)__builtin_amdgcn_workgroup_id_x())

constexpr int S = 2048, D = 512, F = 1792;
constexpr float LN_EPS = 1e-5f;
constexpr int SPIN_CAP = 1 << 17;

typedef float f16v __attribute__((ext_vector_type(16)));
constexpr u32 WS_EPOCH = 0;
constexpr u32 WS_XST = 4096;
constexpr u32 WS_CSG = 20480;
constexpr u32 WS_HST = 36864;
constexpr u32 WS_F1 = 69632;
constexpr u32 WS_FH = WS_F1 + 32768;
constexpr u32 WS_F3 = WS_FH + 65536;
constexpr u32 WS_UX = 262144;
constexpr u32 WS_HF = WS_UX + (4u << 20);
constexpr u32 WS_ACT = WS_HF + (2u << 20);
constexpr u32 WS_WT = WS_ACT + 64u * 114688u;
constexpr u32 WS_END = WS_WT + 448u * 4096u;
constexpr size_t WS_STAMP = 16u << 20;

constexpr u32 L_W1 = 0;
constexpr u32 L_K = 131072;
constexpr u32 L_U = L_K + 8320;
constexpr u32 L_T = 4096;
constexpr u32 L_X = L_T + 9344;
constexpr u32 L_Y = L_U + 9728;
constexpr u32 L_SH = L_W1;
constexpr u32 L_CS = L_Y + 8192;
constexpr u32 L_DUMP = L_CS;
constexpr u32 L_EDGE = L_CS + 512;
constexpr u32 L_CNT = L_EDGE + 320;
constexpr u32 L_TOTAL = 163840;
static_assert(L_CNT + 144 <= L_TOTAL, "lds");
constexpr u32 C_1 = 0, C_2 = 4, C_R1 = 8, C_3 = 12, C_4 = 16, C_4B = 20, C_5 = 24, C_6 = 28, C_CS = 32, C_HA = 36, C_HB = 40,
              C_RA = 44, C_RB = 48, C_ACTA = 52, C_ACTB = 56, C_R3A = 60, C_R3B = 64, C_SA = 68, C_SB = 72, C_PA = 76, C_TAG = 80, C_Q = 84, C_T = 100;

#define ST(i) do {} while (0)
#define STC(i) do {} while (0)
#define STAMP(k, e) do {} while (0)
#define STAMP_ARG
#define STAMP_PASS

DEVI float blo(u32 v) { return u2f(v << 16); }
DEVI float bhi(u32 v) { return u2f(v & 0xffff0000u); }
DEVI u32 pk2(float a, float b) { v2bf p = {(__bf16)a, (__bf16)b}; return __builtin_bit_cast(u32, p); }
DEVI v4u mk_srd(const void* p, u32 bytes) {
  u64 a = (u64)p;
  v4u r = {(u32)a, (u32)(a >> 32) & 0xffffu, bytes, 0x00020000u};
  return r;
}
#define GLD128(dst, srd, voff, soff, imm) \
  asm volatile("buffer_load_dwordx4 %0, %1, %2, %3 offen offset:%4" : "=v"(dst) : "v"(voff), "s"(srd), "s"(soff), "i"(imm) : "memory")
#define GLD128C(dst, srd, voff, soff, imm) \
  asm volatile("buffer_load_dwordx4 %0, %1, %2, %3 offen offset:%4 sc1" : "=v"(dst) : "v"(voff), "s"(srd), "s"(soff), "i"(imm) : "memory")
#define GLD64C(dst, srd, voff, soff, imm) \
  asm volatile("buffer_load_dwordx2 %0, %1, %2, %3 offen offset:%4 sc1" : "=v"(dst) : "v"(voff), "s"(srd), "s"(soff), "i"(imm) : "memory")
#define GLD32(dst, srd, voff, soff, imm) \
  asm volatile("buffer_load_dword %0, %1, %2, %3 offen offset:%4" : "=v"(dst) : "v"(voff), "s"(srd), "s"(soff), "i"(imm) : "memory")
#define GLD32C(dst, srd, voff, soff, imm) \
  asm volatile("buffer_load_dword %0, %1, %2, %3 offen offset:%4 sc1" : "=v"(dst) : "v"(voff), "s"(srd), "s"(soff), "i"(imm) : "memory")
#define GST128C(src, srd, voff, soff, imm) \
  asm volatile("buffer_store_dwordx4 %0, %1, %2, %3 offen offset:%4 sc1\n\ts_nop 1" :: "v"(src), "v"(voff), "s"(srd), "s"(soff), "i"(imm) : "memory")
#define GST32C(src, srd, voff, soff, imm) \
  asm volatile("buffer_store_dword %0, %1, %2, %3 offen offset:%4 sc1" :: "v"(src), "v"(voff), "s"(srd), "s"(soff), "i"(imm) : "memory")
#define DMA16(ldsaddr, srd, voff, soff, imm) \
  asm volatile("s_mov_b32 m0, %0\n\ts_nop 0\n\tbuffer_load_dwordx4 %1, %2, %3 offen offset:%4 lds" \
               :: "s"(ldsaddr), "v"(voff), "s"(srd), "s"(soff), "i"(imm) : "memory")
#define DMA16G(ldsaddr, gptr) \
  asm volatile("s_mov_b32 m0, %0\n\ts_nop 0\n\tglobal_load_lds_dwordx4 %1, off" :: "s"(ldsaddr), "v"(gptr) : "memory")
#define WAITV(n) asm volatile("s_waitcnt vmcnt(%0)" :: "i"(n) : "memory")
#define BAR() asm volatile("s_waitcnt lgkmcnt(0)\n\ts_barrier" ::: "memory")

DEVI v8bf tr_frag(u32 a0, u32 a1) {
  v4s lo = __builtin_amdgcn_ds_read_tr16_b64_v4i16((LAS v4s*)(uintptr_t)a0);
  v4s hi = __builtin_amdgcn_ds_read_tr16_b64_v4i16((LAS v4s*)(uintptr_t)a1);
  v8s r = __builtin_shufflevector(lo, hi, 0, 1, 2, 3, 4, 5, 6, 7);
  return __builtin_bit_cast(v8bf, r);
}
DEVI v4u lds128(u32 a) { return *(LAS v4u*)(uintptr_t)a; }
DEVI f4 ldsf4(u32 a) { return *(LAS f4*)(uintptr_t)a; }
DEVI void sts128(u32 a, v4u v) { *(LAS v4u*)(uintptr_t)a = v; }
DEVI void sts64(u32 a, v2u v) { *(LAS v2u*)(uintptr_t)a = v; }
DEVI void sts32(u32 a, u32 v) { *(LAS u32*)(uintptr_t)a = v; }
DEVI void stsf4(u32 a, f4 v) { *(LAS f4*)(uintptr_t)a = v; }
DEVI f4 mfma16(v8bf a, v8bf b, f4 c) { return __builtin_amdgcn_mfma_f32_16x16x32_bf16(a, b, c, 0, 0, 0); }

template <int CTRL> DEVI float dpp_add(float v) {
  return v + u2f(__builtin_amdgcn_update_dpp(0u, f2u(v), CTRL, 0xf, 0xf, true));
}
DEVI float wave_sum(float v) {
  v = dpp_add<0xb1>(v);
  v = dpp_add<0x4e>(v);
  v = dpp_add<0x141>(v);
  v = dpp_add<0x140>(v);
  auto a = __builtin_amdgcn_permlane16_swap(f2u(v), f2u(v), false, false);
  v = u2f(a[0]) + u2f(a[1]);
  auto b = __builtin_amdgcn_permlane32_swap(f2u(v), f2u(v), false, false);
  return u2f(b[0]) + u2f(b[1]);
}
DEVI v4u row_granule(v4u x, u32 tag) {
  float e[8] = {blo(x.x), bhi(x.x), blo(x.y), bhi(x.y), blo(x.z), bhi(x.z), blo(x.w), bhi(x.w)};
  const float mu = wave_sum(((e[0] + e[1]) + (e[2] + e[3])) + ((e[4] + e[5]) + (e[6] + e[7]))) * (1.f / D);
  float q = 0.f;
#pragma unroll
  for (int j = 0; j < 8; ++j) { const float t = e[j] - mu; q = __builtin_fmaf(t, t, q); }
  const float rs = __builtin_amdgcn_rsqf(wave_sum(q) * (1.f / D) + LN_EPS);
  return v4u{f2u(mu), tag, f2u(rs), ~tag};
}
DEVI bool gran_ok(v4u g, u32 tag) { return g.y == tag && g.w == ~tag; }

DEVI void lds_spin(u32 addr, u32 want) {
  for (int sp = 0; sp < SPIN_CAP * 4; ++sp) {
    if (*(volatile LAS u32*)(uintptr_t)addr >= want) break;
    __builtin_amdgcn_s_sleep(1);
  }
  asm volatile("" ::: "memory");
}
DEVI u32 lds_add(u32 addr) {
  u32 old = 0;
  if ((TID & 63) == 0) old = __hip_atomic_fetch_add((LAS u32*)(uintptr_t)addr, 1u, __ATOMIC_RELAXED, __HIP_MEMORY_SCOPE_WORKGROUP);
  return __builtin_amdgcn_readfirstlane(old);
}
#define POLL_SLEEP 1
DEVI void poll32(v4u srd, u32 voff, u32 soff, u32 tag) {
  for (int sp = 0; sp < SPIN_CAP; ++sp) {
    u32 f;
    asm volatile("buffer_load_dword %0, %1, %2, %3 offen sc1\n\ts_waitcnt vmcnt(0)" : "=v"(f) : "v"(voff), "s"(srd), "s"(soff) : "memory");
    if (ALL(f == tag)) break;
    __builtin_amdgcn_s_sleep(POLL_SLEEP);
  }
}
DEVI void poll256(v4u srd, u32 voff, u32 soff, u32 tag) {
  for (int sp = 0; sp < SPIN_CAP; ++sp) {
    v4u f;
    asm volatile("buffer_load_dwordx4 %0, %1, %2, %3 offen sc1\n\ts_waitcnt vmcnt(0)" : "=v"(f) : "v"(voff), "s"(srd), "s"(soff) : "memory");
    if (ALL(f.x == tag && f.y == tag && f.z == tag && f.w == tag)) break;
    __builtin_amdgcn_s_sleep(POLL_SLEEP);
  }
}

DEVI void poll256l(v4u srd, u32 voff, u32 soff, u32 tag) {
  u32 o0 = voff, o1 = voff + 8192, o2 = voff + 16384, o3 = voff + 24576;
  for (int sp = 0; sp < SPIN_CAP; ++sp) {
    u32 f0, f1, f2, f3;
    asm volatile("buffer_load_dword %0, %4, %8, %9 offen sc1\n\tbuffer_load_dword %1, %5, %8, %9 offen sc1\n\t"
                 "buffer_load_dword %2, %6, %8, %9 offen sc1\n\tbuffer_load_dword %3, %7, %8, %9 offen sc1\n\ts_waitcnt vmcnt(0)"
                 : "=&v"(f0), "=&v"(f1), "=&v"(f2), "=&v"(f3) : "v"(o0), "v"(o1), "v"(o2), "v"(o3), "s"(srd), "s"(soff) : "memory");
    if (f0 == tag) o0 = 0x80000000u;
    if (f1 == tag) o1 = 0x80000000u;
    if (f2 == tag) o2 = 0x80000000u;
    if (f3 == tag) o3 = 0x80000000u;
    if (ALL((o0 & o1 & o2 & o3) >> 31)) break;
    __builtin_amdgcn_s_sleep(POLL_SLEEP);
  }
}
DEVI void poll256m(v4u srd, u32 voff, u32 soff, u32 tag) {
  for (int sp = 0; sp < SPIN_CAP; ++sp) {
    v4u f;
    asm volatile("buffer_load_dwordx4 %0, %1, %2, %3 offen sc1\n\ts_waitcnt vmcnt(0)" : "=v"(f) : "v"(voff), "s"(srd), "s"(soff) : "memory");
    if (f.x == tag && f.y == tag && f.z == tag && f.w == tag) voff = 0x80000000u;
    if (ALL(voff >> 31)) break;
    __builtin_amdgcn_s_sleep(POLL_SLEEP);
  }
}
DEVI void lds_signal(u32 addr) {
  u64 keep;
  asm volatile("s_waitcnt lgkmcnt(0)\n\ts_mov_b64 %0, exec\n\ts_mov_b64 exec, 1\n\tds_add_u32 %1, %2\n\ts_mov_b64 exec, %0"
               : "=&s"(keep) : "v"(addr), "v"(1u) : "memory");
}
DEVI void lds_post(u32 addr) {
  u64 keep;
  asm volatile("s_mov_b64 %0, exec\n\ts_mov_b64 exec, 1\n\tds_add_u32 %1, %2\n\ts_mov_b64 exec, %0" : "=&s"(keep) : "v"(addr), "v"(1u) : "memory");
}
DEVI void lds_wait(u32 addr, u32 want) {
  for (int sp = 0; sp < SPIN_CAP * 16; ++sp)
    if (*(volatile LAS u32*)(uintptr_t)addr >= want) break;
  asm volatile("" ::: "memory");
}
DEVI u32 tag8(v4u g) { return (g.x & 255u) | ((g.y & 255u) << 8) | ((g.z & 255u) << 16) | (g.w << 24); }
DEVI float dot2(u32 a, u32 b, float c) {
  return __builtin_amdgcn_fdot2_f32_bf16(__builtin_bit_cast(v2bf, a), __builtin_bit_cast(v2bf, b), c, false);
}
DEVI float sum8(float v) {
  v = dpp_add<0x128>(v);
  auto a = __builtin_amdgcn_permlane16_swap(f2u(v), f2u(v), false, false);
  v = u2f(a[0]) + u2f(a[1]);
  auto b = __builtin_amdgcn_permlane32_swap(f2u(v), f2u(v), false, false);
  return u2f(b[0]) + u2f(b[1]);
}
DEVI void row_stats(v4u x, float& mu, float& rs) {
  float e[8] = {blo(x.x), bhi(x.x), blo(x.y), bhi(x.y), blo(x.z), bhi(x.z), blo(x.w), bhi(x.w)};
  mu = wave_sum(((e[0] + e[1]) + (e[2] + e[3])) + ((e[4] + e[5]) + (e[6] + e[7]))) * (1.f / D);
  float q = 0.f;
#pragma unroll
  for (int j = 0; j < 8; ++j) { const float t = e[j] - mu; q = __builtin_fmaf(t, t, q); }
  rs = __builtin_amdgcn_rsqf(wave_sum(q) * (1.f / D) + LN_EPS);
}
#define GLD64(dst, srd, voff, soff, imm) \
  asm volatile("buffer_load_dwordx2 %0, %1, %2, %3 offen offset:%4" : "=v"(dst) : "v"(voff), "s"(srd), "s"(soff), "i"(imm) : "memory")
#define GST64(src, srd, voff, soff, imm) \
  asm volatile("buffer_store_dwordx2 %0, %1, %2, %3 offen offset:%4" :: "v"(src), "v"(voff), "s"(srd), "s"(soff), "i"(imm) : "memory")
#define GST64C(src, srd, voff, soff, imm) \
  asm volatile("buffer_store_dwordx2 %0, %1, %2, %3 offen offset:%4 sc1" :: "v"(src), "v"(voff), "s"(srd), "s"(soff), "i"(imm) : "memory")
#define SB() __builtin_amdgcn_sched_barrier(0)
template <int I> struct IC { static constexpr int v = I; };
template <int I, int N, typename FN> DEVI void static_for(FN&& fn) {
  if constexpr (I < N) { fn(IC<I>{}); static_for<I + 1, N>(fn); }
}

#define ST_ARG
#define ST_PASS
DEVI f16v mfma32(v8bf a, v8bf b, f16v c) { return __builtin_amdgcn_mfma_f32_32x32x16_bf16(a, b, c, 0, 0, 0); }
DEVI float pick(u32 m, float a, float b) { return u2f((m & f2u(a)) | (~m & f2u(b))); }
DEVI float rot1(float v) { return u2f(__builtin_amdgcn_mov_dpp(f2u(v), 0x13c, 0xf, 0xf, true)); }
template <int T0, int T1, typename FILL>
DEVI void conv_wave(u32 kbase, u32 ubase, v8bf kd, u32 ybase, u32 ebase, u32 ekeep, u32 l63, FILL fill) {
#define CPF 2
  constexpr int PF = CPF;
  constexpr int ND = T1 + 1, NS = 4 * ND;
  constexpr int TT[2] = {T0, T1};
  v8bf K[4][ND], U[4][ND];
  f16v acc[2];
  float lv[4], nx[4], c1, c2;
  auto loads = [&](auto SI) NOPK {
    constexpr int e = SI.v / ND, d = SI.v % ND;
    U[e][d] = __builtin_bit_cast(v8bf, lds128(ubase + 128 * d + 32 * (3 - e)));
    if constexpr (T1 - d > T0) K[e][T1 - d] = tr_frag(kbase + 128 * (4 * (T1 - d) + e) + 32, kbase + 128 * (4 * (T1 - d) + e));
    if constexpr (d <= T0) K[e][T0 - d] = tr_frag(kbase + 128 * (4 * (T0 - d) + e) + 32, kbase + 128 * (4 * (T0 - d) + e));
  };
#pragma unroll
  for (int t = 0; t < 2; ++t)
#pragma unroll
    for (int i = 0; i < 16; ++i) acc[t][i] = 0.f;
  static_for<0, PF>(loads);
  static_for<0, NS + 5>([&](auto SI) NOPK {
    constexpr int st = SI.v, e = st / ND, d = st % ND;
    if constexpr (st + PF < NS) loads(IC<st + PF>{});
    fill(SI);
    static_for<0, 2>([&](auto TI) NOPK {
      constexpr int t = TI.v, start = t == 1 ? (T1 - T0 >= 3 ? NS : NS + 2) : 3 * ND + T0 + 1;
      constexpr int ph = st - start;
      if constexpr (ph == 0) {
        asm volatile("" : "+v"(acc[t]));
        c2 = acc[t][15];
#pragma unroll
        for (int a = 0; a < 4; ++a) nx[a] = acc[t][4 * a + 2] + rot1(pick(l63, a ? acc[t][(4 * a - 1) & 15] : 0.f, acc[t][4 * a + 3]));
      }
      if constexpr (ph == 1) {
        c1 = nx[3];
#pragma unroll
        for (int a = 0; a < 4; ++a) lv[a] = acc[t][4 * a + 1] + rot1(pick(l63, a ? nx[(a - 1) & 3] : 0.f, nx[a]));
      }
      if constexpr (ph == 2) {
        stsf4(ebase + (TT[t] + 1) * ekeep, f4{lv[3], c1, c2, 0.f});
#pragma unroll
        for (int a = 0; a < 4; ++a) nx[a] = acc[t][4 * a] + rot1(pick(l63, a ? lv[(a - 1) & 3] : 0.f, lv[a]));
        const u32 p0 = pk2(nx[0], nx[1]), p1 = pk2(nx[2], nx[3]);
        *(LAS u16*)(uintptr_t)(ybase + TT[t] * 512) = (u16)p0;
        *(LAS u16*)(uintptr_t)(ybase + TT[t] * 512 + 128) = (u16)(p0 >> 16);
        *(LAS u16*)(uintptr_t)(ybase + TT[t] * 512 + 256) = (u16)p1;
        *(LAS u16*)(uintptr_t)(ybase + TT[t] * 512 + 384) = (u16)(p1 >> 16);
      }
    });
    SB();
    if constexpr (st < NS) {
      if constexpr (d <= T0) acc[0] = mfma32(U[e][d], K[e][T0 - d], acc[0]);
      if constexpr (e == 0 && d == T0) acc[0] = mfma32(U[e][d], kd, acc[0]);
      acc[1] = mfma32(U[e][d], K[e][T1 - d], acc[1]);
      if constexpr (e == 0 && d == T1) acc[1] = mfma32(U[e][d], kd, acc[1]);
    }
    SB();
  });
}

#define CODE_BYTES 41344u
#define G1_WIN 8
#define W1_Q0 4
#define W1_EARLY 10
#define PA_FIRST 1
#define G2_WIN 4
#define G1_UP 8
constexpr int g1_w0 = 18;
constexpr int g1_gj = 27;
constexpr int g1_issued(int j) { return G1_UP + 2 * j < 32 ? G1_UP + 2 * j : 32; }
constexpr int g1_wait(int j) { return j == g1_w0 - 1 ? 0 : g1_issued(j) - (j + 2); }
static_assert((32 - G1_UP) / 2 < g1_w0 - 1, "the h loads are the first 32 operations");

#define QT(o0, o1, a0, b0, a1, b1, m) \
  asm("s_mov_b64 vcc, %6\n\tv_cndmask_b32_dpp %0, %2, %3, vcc row_ror:8 row_mask:0xf bank_mask:0xf\n\t" \
      "v_cndmask_b32_dpp %1, %4, %5, vcc row_ror:8 row_mask:0xf bank_mask:0xf" \
      : "=&v"(o0), "=&v"(o1) : "v"(a0), "v"(b0), "v"(a1), "v"(b1), "s"(m) : "vcc")
constexpr u64 QM = 0x00ff00ff00ff00ffull;
DEVI void gemm1_loop(f16v (&acc)[4], v4u (&w2)[7][4], v4u srdW, u32 l16, u32 hso, u32 dso, u32 wb, v4u& hg, u32 r16, u32 gso, v4u& cg, u32 cso, u32 csl, int rot, bool late, u32 gate, u32 c6 ST_ARG) {
  v4u h[32], ht[32];
  constexpr int UP = G1_UP;
#define G1_GATE 12
  if (G1_GATE && late) lds_spin(gate, 1u);
  u32 hq[4];
#pragma unroll
  for (int q = 0; q < 4; ++q) hq[q] = hso + ((q + rot) & 3) * 8192;
#pragma unroll
  for (int j = 0; j < UP; ++j) GLD128C(h[j], srdW, l16, hq[j >> 3] + ((j >> 2) & 1) * 4096, (j & 3) * 1024);
  if (late) {
#pragma unroll
    for (int q = 14; q < 28; ++q) GLD128C(w2[q >> 2][q & 3], srdW, l16, dso + (q >> 2) * 4096, 1024 * (q & 3));
  }
  lds_wait(c6, 7);
  constexpr int PD = 4;
  v8bf W[128];
  u32 wa[4];
#pragma unroll
  for (int n = 0; n < 4; ++n) wa[n] = wb ^ (32 * n);
#pragma unroll
  for (int f = 0; f < PD; ++f) W[f] = tr_frag(wa[f], wa[f] + 512);
#pragma unroll
  for (int n = 0; n < 4; ++n)
#pragma unroll
    for (int i = 0; i < 16; ++i) acc[n][i] = 0.f;
  u32 qt[4], qo[4];
  {
    constexpr int w0 = UP - 1;
    asm volatile("s_waitcnt vmcnt(%1)" : "+v"(h[0]) : "i"(w0) : "memory");
    auto s0 = __builtin_amdgcn_permlane16_swap(h[0].x, h[0].z, false, false);
    auto s1 = __builtin_amdgcn_permlane16_swap(h[0].y, h[0].w, false, false);
    qt[0] = s0[0]; qt[2] = s0[1]; qt[1] = s1[0]; qt[3] = s1[1];
    asm volatile("s_nop 1" : "+v"(qt[0]), "+v"(qt[1]), "+v"(qt[2]), "+v"(qt[3]));
    QT(qo[0], qo[2], qt[1], qt[0], qt[3], qt[2], QM);
    QT(qo[1], qo[3], qt[0], qt[1], qt[2], qt[3], ~QM);
    ht[0] = v4u{qo[0], qo[1], qo[2], qo[3]};
    asm volatile("s_nop 1" : "+v"(ht[0]));
  }
  ST(8); STC(28);
  static_for<0, 128>([&](auto FI) NOPK {
    constexpr int f = FI.v, j = f >> 2, n = f & 3;
    if constexpr (j + 1 < 32) {
      if constexpr (n == 0) {
        constexpr int wt = g1_wait(j);
        if constexpr (j < g1_w0) asm volatile("s_waitcnt vmcnt(%1)" : "+v"(h[j + 1]) : "i"(wt) : "memory");
        else asm volatile("" : "+v"(h[j + 1]));
        QT(qo[0], qo[2], h[j + 1].y, h[j + 1].x, h[j + 1].w, h[j + 1].z, QM);
      }
      if constexpr (n == 1) QT(qo[1], qo[3], h[j + 1].x, h[j + 1].y, h[j + 1].z, h[j + 1].w, ~QM);
      if constexpr (n == 2) asm("v_permlane16_swap_b32 %0, %1" : "+v"(qo[0]), "+v"(qo[2]));
      if constexpr (n == 3) {
        asm("v_permlane16_swap_b32 %0, %1" : "+v"(qo[1]), "+v"(qo[3]));
        ht[j + 1] = v4u{qo[0], qo[1], qo[2], qo[3]};
      }
    }
    if constexpr (n >= 2 && UP + 2 * j < 32) { constexpr int k = UP + 2 * j + n - 2; GLD128C(h[k], srdW, l16, hq[k >> 3] + ((k >> 2) & 1) * 4096, (k & 3) * 1024); }
    if constexpr (n == 3 && j >= g1_w0) { constexpr int q = j - g1_w0; GLD128C(w2[q >> 2][q & 3], srdW, l16, dso + (q >> 2) * 4096, 1024 * (q & 3)); }
    if constexpr (G1_GATE && n == 3 && j == G1_GATE) sts32(gate, 1u);
    if constexpr (n == 0 && j == g1_gj) GLD128C(hg, srdW, r16, gso, 0);
    if constexpr (n == 1 && j == g1_gj) GLD128C(cg, srdW, l16 & 511u, cso, 0);
    if constexpr (n == 1 && j == 31) {
      asm volatile("s_waitcnt vmcnt(4)" : "+v"(hg), "+v"(cg) :: "memory");
      sts128(csl, cg & ~255u);
    }
    if constexpr (f + PD < 128) {
      if constexpr (j + 1 == 16) { wa[n] += 65536; asm volatile("" : "+v"(wa[n])); }
      W[f + PD] = tr_frag(wa[n] + 4096 * ((j + 1) & 15), wa[n] + 4096 * ((j + 1) & 15) + 512);
    }
    SB();
    acc[n] = __builtin_amdgcn_mfma_f32_32x32x16_bf16(W[f], __builtin_bit_cast(v8bf, ht[j]), acc[n], 0, 0, 0);
    SB();
  });
}
DEVI u32 swiglu2(float ag0, float ag1, float au0, float au1, float cg0, float cg1, float cu0, float cu1, float nmu, float k1, float inv) {
  u32 o;
  float g0, g1, e0, e1, u0, u1;
  asm("v_fma_f32 %1, %15, %11, %7\n\t"
      "v_fma_f32 %2, %15, %12, %8\n\t"
      "v_mul_f32 %3, %16, %1\n\t"
      "v_mul_f32 %4, %16, %2\n\t"
      "v_exp_f32 %3, %3\n\t"
      "v_exp_f32 %4, %4\n\t"
      "v_fma_f32 %5, %15, %13, %9\n\t"
      "v_fma_f32 %6, %15, %14, %10\n\t"
      "v_fma_f32 %3, %3, %17, %17\n\t"
      "v_fma_f32 %4, %4, %17, %17\n\t"
      "v_rcp_f32 %3, %3\n\t"
      "v_rcp_f32 %4, %4\n\t"
      "v_mul_f32 %1, %1, %5\n\t"
      "v_mul_f32 %2, %2, %6\n\t"
      "v_mul_f32 %1, %1, %3\n\t"
      "v_mul_f32 %2, %2, %4\n\t"
      "v_cvt_pk_bf16_f32 %0, %1, %2"
      : "=v"(o), "=&v"(g0), "=&v"(g1), "=&v"(e0), "=&v"(e1), "=&v"(u0), "=&v"(u1)
      : "v"(ag0), "v"(ag1), "v"(au0), "v"(au1), "v"(cg0), "v"(cg1), "v"(cu0), "v"(cu1), "v"(nmu), "v"(k1), "v"(inv));
  return o;
}

typedef u32 v16u __attribute__((ext_vector_type(16)));
NOPK __global__ __launch_bounds__(512, 2) void h3_block(const u16* x_, const u16* k_, const u16* v_, const u16* shift_k_, const u16* ssm_k_,
                                                        const u16* ssm_D_, const u16* wg_, const u16* wu_, const u16* wd_, char* ws_,
                                                        u16* out_ STAMP_ARG) {
  __shared__ __attribute__((aligned(256))) char sm[L_TOTAL];
  STAMP(0, 0);
  v16u ka;
  v4u kb;
  v2u kc;
  {
    const u64 kp = (u64)__builtin_amdgcn_kernarg_segment_ptr();
    asm volatile("s_load_dwordx16 %0, %3, 0x0\n\ts_load_dwordx4 %1, %3, 0x40\n\ts_load_dwordx2 %2, %3, 0x50" : "=&s"(ka), "=&s"(kb), "=&s"(kc) : "s"(kp));
  }
  int bid = BID;
  const int tid = TID, lane = tid & 63;
  const int w = __builtin_amdgcn_readfirstlane(tid >> 6);
  const u32 sb = (u32)(uintptr_t)(LAS char*)sm;
  if (w == 4) {
    u64 pc;
    asm volatile("s_getpc_b64 %0" : "=s"(pc));
    const v4u srdC = {(u32)pc & ~127u, (u32)(pc >> 32) & 0xffffu, CODE_BYTES, 0x00020000u};
    const u32 co = lane < 11 ? (u32)(lane * 32 + (bid >> 3)) * 128 : 0x80000000u;
    asm volatile("s_mov_b32 m0, %0\n\ts_nop 0\n\tbuffer_load_dword %1, %2, 0 offen lds" :: "s"(sb + L_DUMP), "v"(co), "s"(srdC) : "memory");
  }
  asm volatile("s_waitcnt lgkmcnt(0)" : "+s"(ka), "+s"(kb), "+s"(kc));
#define PTR(lo, hi) ((const void*)(((u64)(hi) << 32) | (lo)))
  const void *x = PTR(ka[0], ka[1]), *kin = PTR(ka[2], ka[3]), *vin = PTR(ka[4], ka[5]), *shift_k = PTR(ka[6], ka[7]),
             *ssm_k = PTR(ka[8], ka[9]), *ssm_D = PTR(ka[10], ka[11]), *wg = PTR(ka[12], ka[13]), *wu = PTR(ka[14], ka[15]),
             *wd = PTR(kb[0], kb[1]), *ws = PTR(kb[2], kb[3]), *out = PTR(kc[0], kc[1]);
  ST(0); STC(24);
  const v4u srdW = mk_srd(ws, WS_END);
  const v4u srdX = mk_srd(x, S * D * 2);
  v4u kk[5], vv[2], xx[2], xr[2];
  u32 tagv, dd;
  const u32 l16 = lane * 16;
  const int c8 = tid & 7, rp = tid >> 3;
  if (__builtin_expect(w < 4, 1)) {
    const v4u srdK = mk_srd(kin, S * D * 2), srdV = mk_srd(vin, S * D * 2);
    const u32 vo = (u32)rp * 2048 + c8 * 16, so = (u32)(bid >> 3) * 65536 + (bid & 7) * 128;
    const u32 vk = vo + so - 3072;
    GLD128(kk[0], srdK, vk, 0, 0);
    GLD128(kk[1], srdK, vk + 1024, 0, 0);
    GLD128(kk[2], srdK, vk + 2048, 0, 0);
    GLD128(kk[3], srdK, vo, so, 0); GLD128(kk[4], srdK, vo, so, 1024);
    GLD128(vv[0], srdV, vo, so, 0); GLD128(vv[1], srdV, vo, so, 1024);
    DMA16(sb + L_SH + w * 1024, mk_srd(shift_k, D * 8), l16, (u32)(bid & 7) * 512, 0);
    GLD128(xx[0], srdX, vo, so, 0); GLD128(xx[1], srdX, vo, so, 1024);
  } else {
    const v4u z = {0, 0, 0, 0};
    if (w == 6) sts128(sb + L_U + (lane >> 3) * 1216 + 64 + (lane & 7) * 16, z);
    if (w == 5 && lane < 19)
      sts128(sb + (lane < 9 ? L_CNT + lane * 16 : lane < 17 ? L_K + ((lane - 9) >> 2) * 4160 + ((lane - 9) & 3) * 16 : L_EDGE + (lane - 17) * 144), z);
  }
  asm volatile("s_waitcnt lgkmcnt(0)\n\ts_barrier" : "+s"(bid) :: "memory");
  const int xcd = bid & 7, idx = bid >> 3;
  const u32 cb = sb + L_CNT;
  u32 tag;
  const int n16 = lane & 15, g4 = lane >> 4;
  const int P = xcd * 32 + idx;
  const bool g1 = idx < 28;
  const int i7 = (idx * 37) >> 8;
  const int grp1 = g1 ? (xcd & 4) + i7 : xcd;
  const int sp = 4 * (xcd >> 2) + (idx - 28);
  const int sl = 7 * (xcd & 3) + (g1 ? idx - 7 * i7 : sp);
  const int rot = g1 ? i7 : 0;
  const u32 wvo = (lane >> 3) * (F * 2) + sl * 128 + ((((lane & 7) >> 1) ^ ((((lane >> 4) & 1) << 1) | (w & 1))) * 32) + (lane & 1) * 16;
  const v4u srdM = mk_srd((w & 1) ? wu : wg, (g1 || sp < 7) ? (u32)D * F * 2 : 0u);
  const int wblk = bid * 2 + (w & 1);
  const bool wown = (w >> 1) == 2 && bid < 224;
  v4u wt[4];
  if (__builtin_expect(w < 4, 1)) {
    GLD32(dd, mk_srd(ssm_D, D * 2), (u32)P * 4, 0, 0);
    GLD32(tagv, srdW, 0u, WS_EPOCH, 0);
    u32 sha = sb + L_SH + w * 1024 + c8 * 64;
    u32 ta = sb + L_T + ((8 * c8 + 4 * ((rp >> 1) & 1)) * 36 + 4 * c8 + (rp & 1) * 16 + (rp >> 2)) * 4;
    u32 xa = sb + L_X + c8 * 1104 + rp * 8;
    u32 pa = (tid & 16) ? sb + L_X + (tid >> 5) * 272 + (tid >> 7) * 16 + (tid & 15) * 16
                        : sb + L_T + (2 * (tid >> 5) + ((tid >> 3) & 1)) * 144 + (tid >> 7) * 16 + (tid & 7) * 16;
    u32 ps = (tid & 16) ? 8 * 272 + 32 : 16 * 144 + 32, t16 = tid * 16;
    asm volatile("" : "+v"(sha), "+v"(ta), "+v"(xa), "+v"(pa), "+v"(ps), "+v"(t16));
    ST(22);
    asm volatile("s_waitcnt vmcnt(4)" : "+v"(kk[0]), "+v"(kk[1]), "+v"(kk[2]), "+v"(kk[3]), "+v"(kk[4]), "+v"(vv[0]), "+v"(vv[1]) :: "memory");
    ST(1);
    u32 uu[2][4];
#pragma unroll
    for (int c = 0; c < 4; ++c) {
      const v4u sk = lds128(sha + c * 16);
      const float wa0 = blo(sk.x), wa1 = bhi(sk.x), wa2 = blo(sk.y), wa3 = bhi(sk.y);
      const float wb0 = blo(sk.z), wb1 = bhi(sk.z), wb2 = blo(sk.w), wb3 = bhi(sk.w);
#pragma unroll
      for (int i = 0; i < 2; ++i) {
        float a = blo(kk[i][c]) * wa3; a = __builtin_fmaf(blo(kk[i + 1][c]), wa2, a); a = __builtin_fmaf(blo(kk[i + 2][c]), wa1, a); a = __builtin_fmaf(blo(kk[i + 3][c]), wa0, a);
        float b = bhi(kk[i][c]) * wb3; b = __builtin_fmaf(bhi(kk[i + 1][c]), wb2, b); b = __builtin_fmaf(bhi(kk[i + 2][c]), wb1, b); b = __builtin_fmaf(bhi(kk[i + 3][c]), wb0, b);
        uu[i][c] = pk2(a * blo(vv[i][c]), b * bhi(vv[i][c]));
      }
    }
#pragma unroll
    for (int i = 0; i < 2; ++i)
#pragma unroll
      for (int c = 0; c < 2; ++c) {
        auto sw = __builtin_amdgcn_permlane16_swap(uu[i][c], uu[i][2 + c], false, false);
        sts32(ta + c * 288 + i * 32, __builtin_amdgcn_perm(sw[1], sw[0], 0x05040100u));
        sts32(ta + c * 288 + 144 + i * 32, __builtin_amdgcn_perm(sw[1], sw[0], 0x07060302u));
      }
    asm volatile("s_waitcnt vmcnt(2)" : "+v"(xx[0]), "+v"(xx[1]) :: "memory");
#pragma unroll
    for (int a = 0; a < 4; ++a) wt[a] = v4u{0, 0, 0, 0};
#pragma unroll
    for (int c = 0; c < 4; ++c) sts64(xa + c * 272, v2u{xx[0][c], xx[1][c]});
    lds_signal(cb + C_1);
    lds_wait(cb + C_1, 4);
    ST(21);
    {
      const u32 so = WS_UX + (u32)bid * 16384;
#pragma unroll
      for (int k = 0; k < 4; ++k) {
        const v4u a = lds128(pa + k * ps);
        GST128C(a, srdW, t16, so + k * 4096, 0);
      }
    }
    asm volatile("s_waitcnt vmcnt(0)" : "+v"(dd), "+v"(tagv) :: "memory");
    tag = __builtin_amdgcn_readfirstlane(tagv) + 1u;
    ST(2);
    const u32 arr = lds_add(cb + C_2);
    if (arr == 3) {
      if (lane == 0) GST32C(tag, srdW, (u32)(xcd * 32 + idx) * 128, WS_F1, 0);
#define F1_DELAY 18
      __builtin_amdgcn_s_sleep(F1_DELAY);
      poll32(srdW, (u32)(xcd * 32 + (lane & 31)) * 128, WS_F1, tag);
      asm volatile("s_waitcnt lgkmcnt(0)" ::: "memory");
      if (lane == 0) sts32(cb + C_R1, 1u);
    }
  } else {
    const int ew = w - 4;
    GLD32(tagv, srdW, 0u, WS_EPOCH, 0);
    GLD128(xr[0], srdX, l16, (u32)(bid * 8 + ew * 2) * 1024, 0);
    GLD128(xr[1], srdX, l16, (u32)(bid * 8 + ew * 2) * 1024, 1024);
    GLD32(dd, mk_srd(ssm_D, D * 2), (u32)P * 4, 0, 0);
#define KW_POS 0
    v4u wr[4];
    if (KW_POS < 2)
    {
      const v4u srdS = mk_srd(ssm_k, D * S * 2);
      const u32 kso = (u32)(2 * P + (ew >> 1)) * 4096 + (ew & 1) * 2048, kl = sb + L_K + (ew >> 1) * 4160 + 64 + (ew & 1) * 2048;
      DMA16(kl, srdS, l16, kso, 0); DMA16(kl, srdS, l16, kso, 1024);
    }
    if (KW_POS < 1)
    {
      const int wct = (wblk * 1171) >> 16, wj = wblk - 56 * wct;
      const v4u srdP = mk_srd(wd, wown ? (u32)D * F * 2 : 0u);
      const u32 wvo2 = (u32)(16 * (g4 >> 1) + 4 * (g4 & 1) + 8 * (n16 >> 3)) * 1024 + (n16 & 7) * 16;
#pragma unroll
      for (int i = 0; i < 4; ++i) GLD128(wr[i], srdP, wvo2, (u32)wj * 32768 + wct * 128, i * 1024);
    }
    ST(2);
    asm volatile("s_waitcnt vmcnt(%3)" : "+v"(xr[0]), "+v"(xr[1]), "+v"(tagv) : "i"(1 + (KW_POS < 2 ? 2 : 0) + (KW_POS < 1 ? 4 : 0)) : "memory");
    tag = __builtin_amdgcn_readfirstlane(tagv) + 1u;
    ST(22);
    {
      float m0, r0, m1, r1;
      row_stats(xr[0], m0, r0);
      row_stats(xr[1], m1, r1);
      const v4u gr = {(f2u(m0) & ~255u) | (tag & 255u), (f2u(r0) & ~255u) | ((tag >> 8) & 255u),
                      (f2u(m1) & ~255u) | ((tag >> 16) & 255u), (f2u(r1) & ~255u) | (tag >> 24)};
      const u32 go = lane == 0 ? (u32)(bid * 8 + ew * 2) * 8 : 0x80000000u;
      GST128C(gr, srdW, go, WS_XST, 0);
    }
    ST(1);
    if (KW_POS >= 1) lds_spin(cb + (KW_POS == 3 ? C_1 : C_2), 4u);
    if (KW_POS >= 2)
    {
      const v4u srdS = mk_srd(ssm_k, D * S * 2);
      const u32 kso = (u32)(2 * P + (ew >> 1)) * 4096 + (ew & 1) * 2048, kl = sb + L_K + (ew >> 1) * 4160 + 64 + (ew & 1) * 2048;
      DMA16(kl, srdS, l16, kso, 0); DMA16(kl, srdS, l16, kso, 1024);
    }
    if (KW_POS >= 1)
    {
      const int wct = (wblk * 1171) >> 16, wj = wblk - 56 * wct;
      const v4u srdP = mk_srd(wd, wown ? (u32)D * F * 2 : 0u);
      const u32 wvo2 = (u32)(16 * (g4 >> 1) + 4 * (g4 & 1) + 8 * (n16 >> 3)) * 1024 + (n16 & 7) * 16;
#pragma unroll
      for (int i = 0; i < 4; ++i) GLD128(wr[i], srdP, wvo2, (u32)wj * 32768 + wct * 128, i * 1024);
    }
    asm volatile("s_waitcnt vmcnt(%5)" : "+v"(dd), "+v"(wr[0]), "+v"(wr[1]), "+v"(wr[2]), "+v"(wr[3]) : "i"(KW_POS ? 0 : 1) : "memory");
    u32 lo[4][2], hi[4][2];
#pragma unroll
    for (int i = 0; i < 4; ++i)
#pragma unroll
      for (int d2 = 0; d2 < 2; ++d2) {
        lo[i][d2] = __builtin_amdgcn_update_dpp(wr[i][d2], wr[i][2 + d2], 0x128, 0xf, 0xc, false);
        hi[i][d2] = __builtin_amdgcn_update_dpp(wr[i][2 + d2], wr[i][d2], 0x128, 0xf, 0x3, false);
      }
#pragma unroll
    for (int a = 0; a < 4; ++a) {
      const u32 sel = (a & 1) ? 0x07060302u : 0x05040100u;
      wt[a] = v4u{__builtin_amdgcn_perm(lo[1][a >> 1], lo[0][a >> 1], sel), __builtin_amdgcn_perm(lo[3][a >> 1], lo[2][a >> 1], sel),
                  __builtin_amdgcn_perm(hi[1][a >> 1], hi[0][a >> 1], sel), __builtin_amdgcn_perm(hi[3][a >> 1], hi[2][a >> 1], sel)};
    }
  }

  auto w1_piece = [&](int G) NOPK {
    DMA16(sb + L_W1 + 2048 * G + 1024 * (w & 1), srdM, wvo, (u32)((G + 16 * rot) & 63) * (8 * F * 2), 0);
  };
  {
    const int e = w >> 2;
    u32 kbase = sb + L_K + e * 4160 + 64 - 32 + 32 * g4 + 8 * ((n16 & 3) - (n16 >> 2));
    u32 ubase = sb + L_U + (e * 4 + (lane & 3)) * 1216 + 192 - 96 + 16 * (((lane >> 2) & 7) - (lane >> 5));
    u32 ybase = sb + L_Y + e * 4096 + lane * 2;
    u32 ebase = lane == 63 ? sb + L_EDGE + e * 144 : sb + L_EDGE + 288 + (w & 1) * 16, ekeep = lane == 63 ? 16 : 0;
    u32 l63 = lane == 63 ? ~0u : 0u;
    v4u kd;
    {
      const u32 dv = (lane < 32 && (lane & 3) == 0) ? ((e ? dd >> 16 : dd & 0xffffu) << (((lane >> 2) & 1) * 16)) : 0u;
      kd = v4u{(lane >> 3) == 0 ? dv : 0u, (lane >> 3) == 1 ? dv : 0u, (lane >> 3) == 2 ? dv : 0u, (lane >> 3) == 3 ? dv : 0u};
    }
    u32 uo = (u32)(tid >> 4) * (8 * 16384) + (tid & 15) * 16;
    u32 ul = sb + L_U + ((tid >> 1) & 7) * 1216 + 192 + (tid >> 4) * 32 + (tid & 1) * 16;
    u32 xvo = (u32)(lane >> 4) * (8 * 16384) + n16 * 16, l32 = lane * 32;
    asm volatile("" : "+v"(kbase), "+v"(ubase), "+v"(ybase), "+v"(ebase), "+v"(ekeep), "+v"(l63), "+v"(kd), "+v"(uo), "+v"(ul), "+v"(xvo), "+v"(l32),
                 "+v"(wt[0]), "+v"(wt[1]), "+v"(wt[2]), "+v"(wt[3]));
    lds_wait(cb + C_R1, 1u);
    ST(3);
    v4u ua, xq, st[2];
    GLD128C(ua, srdW, uo, WS_UX + (u32)xcd * 16384 + idx * 512, 0);
    GLD128C(xq, srdW, xvo, WS_UX + (u32)(w * 32 + xcd) * 16384 + idx * 512 + 256, 0);
    GLD128C(st[0], srdW, l32, WS_XST + w * 2048, 0);
    GLD128C(st[1], srdW, l32, WS_XST + w * 2048, 16);
    asm volatile("s_waitcnt vmcnt(3)" : "+v"(ua) :: "memory");
    sts128(ul, ua);
    BAR();
    ST(4); STC(29);
    float q0[4], q1[4];
    auto make_q = [&]() NOPK {
    asm volatile("s_waitcnt vmcnt(0)" : "+v"(xq), "+v"(st[0]), "+v"(st[1]) :: "memory");
    for (int sp = 0; sp < SPIN_CAP; ++sp) {
      if (ALL(tag8(st[0]) == tag && tag8(st[1]) == tag)) break;
      __builtin_amdgcn_s_sleep(2);
      GLD128C(st[0], srdW, l32, WS_XST + w * 2048, 0);
      GLD128C(st[1], srdW, l32, WS_XST + w * 2048, 16);
      asm volatile("s_waitcnt vmcnt(0)" : "+v"(st[0]), "+v"(st[1]) :: "memory");
    }
#pragma unroll
    for (int r = 0; r < 4; ++r) {
      const float mu = u2f(st[r >> 1][2 * (r & 1)] & ~255u), rs = u2f(st[r >> 1][2 * (r & 1) + 1] & ~255u);
      q0[r] = (blo(xq[r]) - mu) * rs;
      q1[r] = (bhi(xq[r]) - mu) * rs;
    }
      asm volatile("" : "+v"(q0[0]), "+v"(q0[1]), "+v"(q0[2]), "+v"(q0[3]), "+v"(q1[0]), "+v"(q1[1]), "+v"(q1[2]), "+v"(q1[3]));
    };
    if (w >= 4) make_q();
    if (wown) {
#pragma unroll
      for (int a = 0; a < 4; ++a) GST128C(wt[a], srdW, l16, WS_WT + (u32)wblk * 4096, a * 1024);
    }
    if (w >= 4) {
#pragma unroll
      for (int m = 0; m < W1_Q0; ++m) w1_piece(2 * m + ((w >> 1) & 1));
    }
    auto fill = [&](auto GI) NOPK {};
    switch (w & 3) {
      case 0: conv_wave<0, 7>(kbase, ubase, __builtin_bit_cast(v8bf, kd), ybase, ebase, ekeep, l63, fill); break;
      case 1: conv_wave<1, 6>(kbase, ubase, __builtin_bit_cast(v8bf, kd), ybase, ebase, ekeep, l63, fill); break;
      case 2: conv_wave<2, 5>(kbase, ubase, __builtin_bit_cast(v8bf, kd), ybase, ebase, ekeep, l63, fill); break;
      default: conv_wave<3, 4>(kbase, ubase, __builtin_bit_cast(v8bf, kd), ybase, ebase, ekeep, l63, fill); break;
    }
    ST(5); STC(30);
    if (w < 4) make_q();
    u32 ya = sb + L_Y + w * 512 + lane * 8;
    u32 ea = sb + L_EDGE + (lane == 0 ? w * 16 : 0);
    u32 hvo = (u32)(lane >> 3) * 32768 + P * 128 + (lane & 7) * 16;
    asm volatile("" : "+v"(q0[0]), "+v"(q0[1]), "+v"(q0[2]), "+v"(q0[3]), "+v"(q1[0]), "+v"(q1[1]), "+v"(q1[2]), "+v"(q1[3]), "+v"(ya),
                 "+v"(ea), "+v"(hvo));
    if (w < 4) {
#pragma unroll
      for (int m = W1_Q0; m < W1_Q0 + W1_EARLY; ++m) w1_piece(2 * m + (w >> 1));
    }
    BAR();
    ST(6); STC(31);
    const v2u y0 = *(LAS v2u*)(uintptr_t)ya, y1 = *(LAS v2u*)(uintptr_t)(ya + 4096);
    const f4 e0 = ldsf4(ea), e1 = ldsf4(ea + 144);
    v4u hv;
#pragma unroll
    for (int r = 0; r < 4; ++r) {
      const float z0 = ((r & 1) ? bhi(y0[r >> 1]) : blo(y0[r >> 1])) + e0[r];
      const float z1 = ((r & 1) ? bhi(y1[r >> 1]) : blo(y1[r >> 1])) + e1[r];
      hv[r] = pk2(__builtin_fmaf(q0[r], z0, blo(xq[r])), __builtin_fmaf(q1[r], z1, bhi(xq[r])));
    }
    GST128C(hv, srdW, hvo, WS_HF + (u32)w * (8 * 32768), 0);
    ST(23);
    WAITV(0);
    BAR();
#define FH_PACK 2
    if (tid == 0) GST32C(tag, srdW, FH_PACK == 0 ? (u32)bid * 128 : FH_PACK == 1 ? (u32)(bid >> 2) * 128 + (bid & 3) * 4 : (u32)(bid & 63) * 128 + (bid >> 6) * 4, WS_FH, 0);
    ST(7);
    constexpr int G0 = 2 * (W1_Q0 + W1_EARLY);
    if (w & 1) {
#pragma unroll
      for (int m = 0; m < (64 - G0) / 4; ++m) w1_piece(G0 + 4 * m + (w >> 1));
    } else if (w) {
#pragma unroll
      for (int m = 0; m < (64 - G0) / 3; ++m) w1_piece(G0 + 3 * m + (w >> 1) - 1);
    }
    static_assert((64 - G0) % 12 == 0, "pieces");
  }

  const int half = w >> 2, wq = w & 3;
  const int row0 = 256 * grp1 + 64 * wq + 32 * half;
#define FH_DELAY 26
  if (w == 0) {
    __builtin_amdgcn_s_sleep(FH_DELAY);
    poll256m(srdW, (u32)lane * 128, WS_FH, tag);
    if (lane == 0) sts32(cb + C_RA, 1u);
    if (bid == 0 && lane == 0) GST32C(tag, srdW, 0u, WS_EPOCH, 0);
  } else {
    WAITV(0);
    lds_signal(cb + C_6);
  }
  ST(9);
  if (!g1 && sp < 7 && half) {
      const int tl = wq;
      lds_wait(cb + C_6, 7);
      const int blk = (lane >> 4) & 1, q = n16 >> 2;
      u32 wa = sb + L_W1 + 2048 * (lane >> 5) + 1024 * blk + 128 * q + 8 * (lane & 3) + 32 * (tl ^ ((((q >> 1) & 1) << 1) | blk));
      const v8bf ones = __builtin_bit_cast(v8bf, v4u{0x3f803f80u, 0x3f803f80u, 0x3f803f80u, 0x3f803f80u});
      f16v c;
#pragma unroll
      for (int i = 0; i < 16; ++i) c[i] = 0.f;
      v8bf fr[32];
#pragma unroll
      for (int j = 0; j < 4; ++j) fr[j] = tr_frag(wa + 4096 * j, wa + 4096 * j + 512);
#pragma unroll
      for (int j = 0; j < 32; ++j) {
        if (j == 12) { wa += 65536; asm volatile("" : "+v"(wa)); }
        if (j + 4 < 32) fr[j + 4] = tr_frag(wa + 4096 * ((j + 4) & 15), wa + 4096 * ((j + 4) & 15) + 512);
        SB();
        c = __builtin_amdgcn_mfma_f32_32x32x16_bf16(fr[j], ones, c, 0, 0, 0);
        SB();
      }
      const u32 ca = sb + L_Y + wq * 128;
      if ((lane & 31) == 0) {
#pragma unroll
        for (int r = 0; r < 4; ++r) stsf4(ca + 32 * r + 16 * (lane >> 5), f4{c[4 * r], c[4 * r + 1], c[4 * r + 2], c[4 * r + 3]});
      }
      const v4u t = lds128(ca + (lane & 7) * 16);
      const v4u g = {(t.x & ~255u) | (tag & 255u), (t.y & ~255u) | ((tag >> 8) & 255u), (t.z & ~255u) | ((tag >> 16) & 255u), (t.w & ~255u) | (tag >> 24)};
      if (lane < 8) GST128C(g, srdW, l16, WS_CSG + sl * 512 + tl * 128, 0);
  }
  lds_wait(cb + C_RA, 1u);
  ST(10); STC(25);
  __builtin_amdgcn_s_setprio(1);
  v4u w2[7][4];
  const int ct = idx & 7, rsub = idx >> 3;
  const int hh = lane >> 5;
  const u32 r16 = (u32)(4 * (lane & 7) + ((lane >> 3) & 3)) * 16;
  const u32 dso = WS_WT + (u32)(ct * 56 + 7 * w) * 4096;
  if (g1) {
    f16v acc[4];
    v4u hg, cg;
    const u32 gso = WS_HST + (u32)row0 * 16, cso = WS_CSG + sl * 512, csl = sb + L_Y + w * 512;
    {
      const u32 wb = sb + L_W1 + 2048 * hh + 1024 * (g4 & 1) + 128 * (n16 >> 2) + 8 * (lane & 3) + 32 * (((n16 >> 3) << 1) | (g4 & 1));
      gemm1_loop(acc, w2, srdW, l16, WS_HF + (u32)(row0 >> 5) * 32768, dso, wb, hg, r16, gso, cg, cso, csl + (l16 & 511u), rot, half != 0, cb + C_T + 4 * wq, cb + C_6 ST_PASS);
    }
    asm volatile("" : "+v"(acc[0]), "+v"(acc[1]), "+v"(acc[2]), "+v"(acc[3]));
    ST(11); STC(26);
    if (!ALL(gran_ok(hg, tag) && tag8(cg) == tag)) {
      for (int sp = 0; sp < SPIN_CAP; ++sp) {
        GLD128C(hg, srdW, r16, gso, 0);
        GLD128C(cg, srdW, l16 & 511u, cso, 0);
        asm volatile("s_waitcnt vmcnt(0)" : "+v"(hg), "+v"(cg) :: "memory");
        if (ALL(gran_ok(hg, tag) && tag8(cg) == tag)) break;
        __builtin_amdgcn_s_sleep(2);
      }
      sts128(csl + (l16 & 511u), cg & ~255u);
    }
    float rs = u2f(hg.z), nmu = -u2f(hg.x);
    float k1 = rs * -1.44269504088896341f, inv = __builtin_amdgcn_rcpf(rs * rs);
    asm volatile("" : "+v"(nmu), "+v"(k1), "+v"(inv));
#define PEA 0
    if (half) __builtin_amdgcn_s_setprio(2); else __builtin_amdgcn_s_setprio(PEA);
    ST(14);
    const u32 aso = WS_ACT + (u32)(row0 >> 5) * 114688 + sl * 4096;
    asm volatile("s_nop 7\n\ts_nop 7" ::: "memory");
#pragma unroll
    for (int n = 0; n < 4; ++n) {
      const u32 ca = csl + 128 * n + 16 * hh;
      const f4 cg0 = ldsf4(ca), cg1 = ldsf4(ca + 32), cu0 = ldsf4(ca + 64), cu1 = ldsf4(ca + 96);
      v4u o;
      o.x = swiglu2(acc[n][0], acc[n][1], acc[n][8], acc[n][9], cg0[0], cg0[1], cu0[0], cu0[1], nmu, k1, inv);
      o.y = swiglu2(acc[n][2], acc[n][3], acc[n][10], acc[n][11], cg0[2], cg0[3], cu0[2], cu0[3], nmu, k1, inv);
      o.z = swiglu2(acc[n][4], acc[n][5], acc[n][12], acc[n][13], cg1[0], cg1[1], cu1[0], cu1[1], nmu, k1, inv);
      o.w = swiglu2(acc[n][6], acc[n][7], acc[n][14], acc[n][15], cg1[2], cg1[3], cu1[2], cu1[3], nmu, k1, inv);
      GST128C(o, srdW, l16, aso + n * 1024, 0);
      if (!half && n < 3) {
#pragma unroll
        for (int q = 14 + n * 5; q < (n == 2 ? 28 : 14 + n * 5 + 5); ++q) GLD128C(w2[q >> 2][q & 3], srdW, l16, dso + (q >> 2) * 4096, 1024 * (q & 3));
      }
    }
    ST(12);
    WAITV(0);
    if (lds_add(cb + (half ? C_ACTB : C_ACTA)) == 3) {
      if (lane == 0) GST32C(tag, srdW, (u32)(grp1 * 32 + sl) * 128, WS_F3 + half * 32768, 0);
    }
    __builtin_amdgcn_s_setprio(0);
  } else {
    v4u sr[8];
    const int r32 = 256 * xcd + 64 * (idx - 28) + 32 * half;
#pragma unroll
    for (int i = 0; i < 8; ++i) GLD128C(sr[i], srdW, l16, WS_HF + (u32)(r32 >> 5) * 32768 + wq * 8192 + (i >> 2) * 4096, (i & 3) * 1024);
    const u32 sa = sb + L_U + half * 8192;
    f4 s1 = {0.f, 0.f, 0.f, 0.f}, s2 = {0.f, 0.f, 0.f, 0.f};
#pragma unroll
    for (int i = 0; i < 8; ++i) {
      asm volatile("s_waitcnt vmcnt(%1)" : "+v"(sr[i]) : "i"(7 - i) : "memory");
#pragma unroll
      for (int r = 0; r < 4; ++r) { s1[r] = dot2(sr[i][r], 0x3f803f80u, s1[r]); s2[r] = dot2(sr[i][r], sr[i][r], s2[r]); }
    }
    stsf4(sa + wq * 1024 + l16, s1);
    stsf4(sa + 4096 + wq * 1024 + l16, s2);
    BAR();
    if (wq == 0) {
      float m = 0.f, q = 0.f;
#pragma unroll
      for (int i = 0; i < 16; ++i) {
        m += *(LAS float*)(uintptr_t)(sa + (2 * i + hh) * 128 + (lane & 31) * 4);
        q += *(LAS float*)(uintptr_t)(sa + 4096 + (2 * i + hh) * 128 + (lane & 31) * 4);
      }
      auto ms = __builtin_amdgcn_permlane32_swap(f2u(m), f2u(m), false, false);
      auto qs = __builtin_amdgcn_permlane32_swap(f2u(q), f2u(q), false, false);
      m = (u2f(ms[0]) + u2f(ms[1])) * (1.f / D);
      q = (u2f(qs[0]) + u2f(qs[1])) * (1.f / D);
      const v4u gr = {f2u(m), tag, f2u(__builtin_amdgcn_rsqf(__builtin_fmaxf(q - m * m, 0.f) + LN_EPS)), ~tag};
      if (lane < 32) GST128C(gr, srdW, l16, WS_HST + (u32)r32 * 16, 0);
    }
    ST(11); STC(26);
#pragma unroll
    for (int J = 0; J < 7; ++J)
#pragma unroll
      for (int i = 0; i < 4; ++i) GLD128C(w2[J][i], srdW, l16, dso + J * 4096, 1024 * i);
    ST(12);
  }
  ST(13);
  u32 avo = (u32)g4 * 512 + n16 * 16;
  u32 pwo = sb + w * 8192 + (n16 >> 1) * 512 + (g4 >> 1) * 256 + (n16 & 1) * 128;
  u32 pwx = (u32)(((g4 & 1) * 4) ^ (n16 & 7)) * 16;
  const int pos = 8 * wq + (lane >> 3), orow = 4 * (pos & 7) + 2 * (pos >> 4) + ((pos >> 3) & 1);
  u32 pro = sb + half * 65536 + (pos >> 1) * 512 + (pos & 1) * 128 + (((lane & 7) ^ (pos & 7)) * 16);
  u32 ovo = (u32)orow * 1024 + ct * 128 + (lane & 7) * 16;
  u32 rvo = (u32)(ct * 32 + 4 * (lane & 7)) * 128 + orow * 4;
  const int t32 = 8 * xcd + 2 * rsub;
  v4u hr;
  {
    u32 h0, h1, h2, h3;
    GLD32C(h0, srdW, rvo, WS_HF + (u32)(t32 + half) * 32768, 0);
    GLD32C(h1, srdW, rvo, WS_HF + (u32)(t32 + half) * 32768, 128);
    GLD32C(h2, srdW, rvo, WS_HF + (u32)(t32 + half) * 32768, 256);
    GLD32C(h3, srdW, rvo, WS_HF + (u32)(t32 + half) * 32768, 384);
    asm volatile("" : "+v"(avo), "+v"(pwo), "+v"(pwx), "+v"(pro), "+v"(ovo));
    const v4u srdO = mk_srd(out, S * D * 2);
    const u32 aso = WS_ACT + (u32)t32 * 114688 + w * 14336;
    v4u av[7][2];
    f4 o2[2][2][4];
#pragma unroll
    for (int hf = 0; hf < 2; ++hf) {
      if (w == 0) {
        __builtin_amdgcn_s_setprio(3);
#define F3_DELAY 20
        if (hf == 0) __builtin_amdgcn_s_sleep(F3_DELAY);
        poll32(srdW, (u32)(xcd * 32 + (lane < 28 ? lane : 0)) * 128, WS_F3 + hf * 32768, tag);
        if (lane == 0) sts32(cb + (hf ? C_R3B : C_R3A), 1u);
      } else if (hf == 0) {
        lds_spin(cb + C_R3A, 1u);
      } else {
        lds_wait(cb + C_R3B, 1u);
      }
      ST(15 + 3 * hf);
#pragma unroll
      for (int J = 0; J < 7; ++J) {
        GLD128C(av[J][0], srdW, avo, aso + hf * 114688 + J * 2048, 0);
        GLD128C(av[J][1], srdW, avo, aso + hf * 114688 + J * 2048, 256);
      }
      if (hf == 1 && (PA_FIRST == 0 || (PA_FIRST == 2 && half))) {
        if (g1) lds_wait(cb + C_ACTB, 4u);
#pragma unroll
        for (int t = 0; t < 2; ++t)
#pragma unroll
          for (int r = 0; r < 4; ++r)
            stsf4(pwo + t * 4096 + (pwx ^ (r * 16)), f4{o2[0][t][0][r], o2[0][t][1][r], o2[0][t][2][r], o2[0][t][3][r]});
        ST(17);
      }
#pragma unroll
      for (int t = 0; t < 2; ++t)
#pragma unroll
        for (int a = 0; a < 4; ++a) o2[hf][t][a] = f4{0.f, 0.f, 0.f, 0.f};
#pragma unroll
      for (int J = 0; J < 7; ++J) {
        asm volatile("s_waitcnt vmcnt(%2)" : "+v"(av[J][0]), "+v"(av[J][1]) : "i"(2 * (6 - J)) : "memory");
        if (hf == 0) asm volatile("" : "+v"(w2[J][0]), "+v"(w2[J][1]), "+v"(w2[J][2]), "+v"(w2[J][3]));
        if (hf == 0 && J == 0) { asm volatile("" : "+v"(h0), "+v"(h1), "+v"(h2), "+v"(h3)); hr = v4u{h0, h1, h2, h3}; }
#pragma unroll
        for (int t = 0; t < 2; ++t)
#pragma unroll
          for (int a = 0; a < 4; ++a) o2[hf][t][a] = mfma16(__builtin_bit_cast(v8bf, w2[J][a]), __builtin_bit_cast(v8bf, av[J][t]), o2[hf][t][a]);
        SB();
      }
#pragma unroll
      for (int t = 0; t < 2; ++t) asm volatile("" : "+v"(o2[hf][t][0]), "+v"(o2[hf][t][1]), "+v"(o2[hf][t][2]), "+v"(o2[hf][t][3]));
      ST(16 + 3 * hf);
      if (hf == 0 && (PA_FIRST == 1 || (PA_FIRST == 2 && !half))) {
        if (g1) lds_wait(cb + C_ACTB, 4u);
#pragma unroll
        for (int t = 0; t < 2; ++t)
#pragma unroll
          for (int r = 0; r < 4; ++r)
            stsf4(pwo + t * 4096 + (pwx ^ (r * 16)), f4{o2[0][t][0][r], o2[0][t][1][r], o2[0][t][2][r], o2[0][t][3][r]});
        ST(17);
      }
    }
#pragma unroll
    for (int t = 0; t < 2; ++t)
#pragma unroll
      for (int r = 0; r < 4; ++r)
        stsf4(pwo + 65536 + t * 4096 + (pwx ^ (r * 16)), f4{o2[1][t][0][r], o2[1][t][1][r], o2[1][t][2][r], o2[1][t][3][r]});
    BAR();
    f4 lo = ldsf4(pro), hi = ldsf4(pro + 256);
#pragma unroll
    for (int i = 1; i < 8; ++i) { lo += ldsf4(pro + i * 8192); hi += ldsf4(pro + i * 8192 + 256); }
    const v4u res = {pk2(lo[0] + blo(hr.x), lo[1] + bhi(hr.x)), pk2(lo[2] + blo(hr.y), lo[3] + bhi(hr.y)),
                     pk2(hi[0] + blo(hr.z), hi[1] + bhi(hr.z)), pk2(hi[2] + blo(hr.w), hi[3] + bhi(hr.w))};
    GST128C(res, srdO, ovo, (u32)(t32 + half) * 32768, 0);
  }
  ST(20); STC(27);
  STAMP(0, 1);
}

extern "C" void kernel_launch(void* const* d_in, const int* in_sizes, int n_in, void* d_out, int out_size, void* d_ws,
                              size_t ws_size, hipStream_t stream) {
  char* ws = (char*)d_ws;
  h3_block<<<256, 512, 0, stream>>>((const u16*)d_in[0], (const u16*)d_in[1], (const u16*)d_in[2], (const u16*)d_in[3],
                                    (const u16*)d_in[4], (const u16*)d_in[5], (const u16*)d_in[7], (const u16*)d_in[8],
                                    (const u16*)d_in[9], ws, (u16*)d_out STAMP_PASS);
}
```
